```python
import torch

def _softmax(x, axis=-1):
    return torch.softmax(x, dim=axis)
def _gelu(x):
    return torch.nn.functional.gelu(x, approximate="tanh")
def _silu(x):
    return torch.nn.functional.silu(x)
def _sigmoid(x):
    return torch.sigmoid(x)
def _layernorm(x, axis=-1, eps=1e-5):
    m = x.mean(dim=axis, keepdim=True)
    v = x.var(dim=axis, keepdim=True, unbiased=False)
    return (x - m) / torch.sqrt(v + eps)
def _scatter_add(tgt, idx, src, axis=0):
    return tgt.clone().index_add_(axis, idx, src)
def _segment_sum(src, seg, n):
    out = torch.zeros((n,) + src.shape[1:], dtype=src.dtype)
    return out.index_add_(0, seg, src)
def _pad(x, pw):
    flat = tuple(v for pair in reversed(pw) for v in pair)
    return torch.nn.functional.pad(x, flat)

kernel_name = 'gated_mix__linear_attn_block__glu_gated_glu__prompt'

def setup_inputs():

    _w = (lambda a: (lambda t: t.to(torch.bfloat16) if t.is_floating_point() else t)(torch.as_tensor(a)))
    def _A_setup():
        B, S, D = 1, 2048, 640
        return {
            "x": _w((lambda *s: torch.randn(*s, dtype=torch.bfloat16) * 0.05)(B, S, D)),
            "wqkv": _w((lambda *s: torch.randn(*s, dtype=torch.bfloat16) * 0.8)(D, 3 * D)),
            "wo": _w((lambda *s: torch.randn(*s, dtype=torch.bfloat16) * 0.2)(D, D)),
        }
    _A_in = _A_setup()

    def _B_setup():
        B, S, D, F = 1, 2048, 640, 1728
        return {
            "x": _w((lambda *s: torch.randn(*s, dtype=torch.bfloat16) * 0.05)(B, S, D)),
            "w_gate": _w((lambda *s: torch.randn(*s, dtype=torch.bfloat16) * 0.4)(D, F)),
            "w_up": _w((lambda *s: torch.randn(*s, dtype=torch.bfloat16) * 0.05)(D, F)),
            "w_down": _w((lambda *s: torch.randn(*s, dtype=torch.bfloat16) * 0.05)(F, D)),
        }
    _B_in = _B_setup()

    return {
        "A_x": _A_in["x"],
        "A_wqkv": _A_in["wqkv"],
        "A_wo": _A_in["wo"],
        "B_x": _B_in["x"],
        "B_w_gate": _B_in["w_gate"],
        "B_w_up": _B_in["w_up"],
        "B_w_down": _B_in["w_down"],
        "gate_w": _w((lambda *s: torch.randn(*s, dtype=torch.bfloat16) * 0.05)(640) * 16.0),
    }

def reference(A_x, A_wqkv, A_wo, B_x, B_w_gate, B_w_up, B_w_down, gate_w):
    def _A_ref(x, wqkv, wo):
        B, S, D = x.shape
        H = 32; Dh = D // H
        qkv = (lambda x, *d: x.permute(*d))((x @ wqkv).reshape(B, S, 3, H, Dh), 2, 0, 3, 1, 4)
        q, k, v = qkv[0], qkv[1], qkv[2]
        phi = lambda t: torch.where(t > 0, t + 1, torch.exp(t)) + 1e-6
        pq, pk = phi(q), phi(k)
        kv = torch.einsum("bhsd,bhse->bhde", pk, v)
        z = torch.einsum("bhsd->bhd", pk)
        num = torch.einsum("bhqd,bhde->bhqe", pq, kv)
        den = torch.einsum("bhqd,bhd->bhq", pq, z)[..., None] + 1e-6
        o = num / den
        return (lambda x, *d: x.permute(*d))(o, 0, 2, 1, 3).reshape(B, S, D) @ wo

    def _B_ref(x, w_gate, w_up, w_down):
        g = torch.einsum("bsd,df->bsf", x, w_gate)
        u = torch.einsum("bsd,df->bsf", x, w_up)
        h = _sigmoid(g) * u
        return torch.einsum("bsf,fd->bsd", h, w_down)

    x = A_x
    g = _sigmoid((x * gate_w).sum(-1, keepdims=True))
    a = _A_ref(x, A_wqkv, A_wo)
    b = _B_ref(x, B_w_gate, B_w_up, B_w_down)
    return g * a + (1 - g) * b
```

```cpp
#include <hip/hip_runtime.h>
#include <hip/hip_bf16.h>
typedef unsigned short u16;
__device__ __forceinline__ float b2f(u16 b){ return __builtin_bit_cast(float, (unsigned)b<<16); }
__device__ __forceinline__ u16 f2b(float f){ __hip_bfloat16 h = __float2bfloat16(f); return __builtin_bit_cast(u16,h); }
constexpr int S=2048, D=640, F=1728, H=32, DH=20;
__global__ void gemm(const u16* A, const u16* B, float* C, int M, int N, int K){
  int n = blockIdx.x*64 + (threadIdx.x&63); int m = blockIdx.y*4 + (threadIdx.x>>6);
  if(n>=N||m>=M) return;
  float acc=0; for(int k=0;k<K;k++) acc += b2f(A[m*K+k])*b2f(B[k*N+n]);
  C[m*N+n]=acc;
}
__device__ float phi(float t){ return (t>0? t+1.f : __expf(t)) + 1e-6f; }
__global__ void kvk(const float* qkv, float* kv, float* z){
  int h=blockIdx.x; int t=threadIdx.x; if(t>=420) return;
  int d=t/21, e=t%21; float acc=0;
  for(int s=0;s<S;s++){ float pk=phi(qkv[s*1920+640+h*20+d]); float v = e<20? qkv[s*1920+1280+h*20+e]:1.f; acc+=pk*v; }
  if(e<20) kv[h*400+d*20+e]=acc; else z[h*20+d]=acc;
}
__global__ void ok(const float* qkv, const float* kv, const float* z, u16* o){
  int s=blockIdx.x; int c=threadIdx.x; int h=c/20,e=c%20;
  float num=0,den=0; for(int d=0;d<20;d++){ float pq=phi(qkv[s*1920+h*20+d]); num+=pq*kv[h*400+d*20+e]; den+=pq*z[h*20+d]; }
  o[s*640+c]=f2b(num/(den+1e-6f));
}
__global__ void hk(const float* g, const float* u, u16* h){ int i=blockIdx.x*256+threadIdx.x; h[i]=f2b(u[i]/(1.f+__expf(-g[i]))); }
__global__ void fin(const u16* x,const u16* gw,const float* a,const float* b,u16* out){
  int s=blockIdx.x; int c=threadIdx.x; float acc=0; for(int k=0;k<640;k++) acc+=b2f(x[s*640+k])*b2f(gw[k]);
  float g=1.f/(1.f+__expf(-acc)); out[s*640+c]=f2b(g*a[s*640+c]+(1-g)*b[s*640+c]);
}
extern "C" void kernel_launch(void* const* d_in, const int* in_sizes, int n_in, void* d_out, int out_size, void* d_ws, size_t ws_size, hipStream_t stream) {
  const u16* x=(const u16*)d_in[0]; const u16* wqkv=(const u16*)d_in[1]; const u16* wo=(const u16*)d_in[2];
  const u16* wg=(const u16*)d_in[4]; const u16* wu=(const u16*)d_in[5]; const u16* wd=(const u16*)d_in[6]; const u16* gw=(const u16*)d_in[7];
  float* qkv=(float*)d_ws; float* g=qkv+S*1920; float* u=g+S*F; float* a=u+S*F; float* b=a+S*D; float* kv=b+S*D; float* z=kv+12800; u16* o=(u16*)(z+640); u16* h=o+S*D;
  gemm<<<dim3(30,512),256,0,stream>>>(x,wqkv,qkv,S,1920,D);
  gemm<<<dim3(27,512),256,0,stream>>>(x,wg,g,S,F,D);
  gemm<<<dim3(27,512),256,0,stream>>>(x,wu,u,S,F,D);
  kvk<<<32,448,0,stream>>>(qkv,kv,z);
  ok<<<S,640,0,stream>>>(qkv,kv,z,o);
  hk<<<S*F/256,256,0,stream>>>(g,u,h);
  gemm<<<dim3(10,512),256,0,stream>>>(o,wo,a,S,D,D);
  gemm<<<dim3(10,512),256,0,stream>>>(h,wd,b,S,D,F);
  fin<<<S,640,0,stream>>>(x,gw,a,b,(u16*)d_out);
}
```

```cpp
#include <hip/hip_runtime.h>
#include <stdint.h>

typedef unsigned short u16;
typedef unsigned int u32;
typedef unsigned long long u64;
typedef short s4 __attribute__((ext_vector_type(4)));
typedef short s8 __attribute__((ext_vector_type(8)));
typedef __bf16 bf8 __attribute__((ext_vector_type(8)));
typedef __bf16 bf2 __attribute__((ext_vector_type(2)));
typedef float f2 __attribute__((ext_vector_type(2)));
typedef float f4 __attribute__((ext_vector_type(4)));
typedef u32 u4 __attribute__((ext_vector_type(4)));
typedef u32 u2 __attribute__((ext_vector_type(2)));
#define LAS __attribute__((address_space(3)))
#define GAS __attribute__((address_space(1)))
#define DEV __device__ __forceinline__
#define SB0() __builtin_amdgcn_sched_barrier(0)

constexpr int S_ = 2048, D_ = 640, F_ = 1728;
constexpr int KS2 = 74, KH = 54;
constexpr u32 AF_OFF = 0, AF_BYTES = 32u * KS2 * 4096u;
constexpr u32 BF_OFF = AF_OFF + AF_BYTES, BF_BYTES = 8u * KS2 * 5120u;
constexpr u32 WOT_OFF = BF_OFF + BF_BYTES, WOT_BYTES = 32u * 40u * 1024u;
constexpr u32 KVP_OFF = WOT_OFF + WOT_BYTES, KVP_BYTES = 8u * 32u * 20u * 24u * 4u;
constexpr u32 ZP_OFF = KVP_OFF + KVP_BYTES, ZP_BYTES = 8u * 640u * 4u;
constexpr u32 GS_OFF = ZP_OFF + ZP_BYTES, GS_BYTES = 2048u * 4u;
constexpr u32 FLAG_OFF = GS_OFF + GS_BYTES, FLAG_BYTES = 256u * 64u;
constexpr u32 WS_BYTES = FLAG_OFF + FLAG_BYTES;
#define STAMP(k, i) do {} while (0)
#define STAMPC(k, i) do {} while (0)

constexpr u32 XBUF = 32768, WRING = 3 * XBUF, WSLOT = 12288, K1_LDS = WRING + 5 * WSLOT;
constexpr u32 K2_LDS = 81920 + 4 * 2560;

DEV u4 make_srd(const void* p, u32 bytes) {
  u64 a = (u64)p;
  u4 r = {(u32)a, (u32)(a >> 32) & 0xffffu, bytes, 0x00020000u};
  return r;
}
DEV void dma16(u32 lds, u32 voff, u4 srd, u32 soff) {
  asm volatile("s_mov_b32 m0, %0\n\ts_nop 0\n\tbuffer_load_dwordx4 %1, %2, %3 offen lds" ::"s"(lds), "v"(voff), "s"(srd), "s"(soff) : "memory");
}
DEV s4 trread(u32 a) { return __builtin_amdgcn_ds_read_tr16_b64_v4i16((LAS s4*)a); }
DEV bf8 cat8(s4 lo, s4 hi) { return __builtin_bit_cast(bf8, __builtin_shufflevector(lo, hi, 0, 1, 2, 3, 4, 5, 6, 7)); }
DEV f4 mfma(bf8 a, bf8 b, f4 c) { return __builtin_amdgcn_mfma_f32_16x16x32_bf16(a, b, c, 0, 0, 0); }
DEV f4 mfma(u4 a, u4 b, f4 c) { return mfma(__builtin_bit_cast(bf8, a), __builtin_bit_cast(bf8, b), c); }
DEV u32 pk2(float a, float b) { f2 v = {a, b}; return __builtin_bit_cast(u32, __builtin_convertvector(v, bf2)); }
DEV u2 pk4(f4 v) { u2 r = {pk2(v[0], v[1]), pk2(v[2], v[3])}; return r; }
DEV float bflo(u32 v) { return __builtin_bit_cast(float, v << 16); }
DEV float bfhi(u32 v) { return __builtin_bit_cast(float, v & 0xffff0000u); }
DEV float sigm(float x) { return __builtin_amdgcn_rcpf(1.f + __builtin_amdgcn_exp2f(-1.44269504089f * x)); }
DEV float phi(float t) { return (t > 0.f ? t + 1.f : __builtin_amdgcn_exp2f(1.44269504089f * t)) + 1e-6f; }
#define LGKM0() __builtin_amdgcn_s_waitcnt(0xC07F)
#define VMCNT(n) asm volatile("s_waitcnt vmcnt(" #n ")" ::: "memory")
#define BARRIER() do { asm volatile("" ::: "memory"); __builtin_amdgcn_s_barrier(); asm volatile("" ::: "memory"); } while (0)

struct Loop {
  u4 xsrd, wsrd;
  u32 xnrec, wnrec, xso, wso, wpitch64;
  u32 xvo[2], wvo[3];
  u32 xdst, wdst, whalf;
  u32 xrd[2], wrd[3];
};

template <bool TR, int H>
DEV void half_step(f4 (&acc)[4][6], const bf8 (&xc)[4], const bf8 (&wc)[6], bf8 (&xn)[4], bf8 (&wn)[6], u32 xa, const u32 (&wa)[3],
                   const Loop& L, u4 srd, u32 dst, u32 so) {
  s4 lo[6];
#pragma unroll
  for (int k = 0; k < 24; ++k) {
    const int b = k >> 2, i = k & 3;
    acc[i][b] = TR ? mfma(wc[b], xc[i], acc[i][b]) : mfma(xc[i], wc[b], acc[i][b]);
    SB0();
    if (k < 4) xn[k] = *(LAS bf8*)(xa + k * 2048);
    else if (k < 16) {
      const int r = k - 4, nb = r >> 1;
      const u32 a = wa[nb % 3] + (nb / 3) * 6144 + (r & 1) * 768;
      if (r & 1) wn[nb] = cat8(lo[nb], trread(a)); else lo[nb] = trread(a);
    }
    if (k % 6 == 1) {
      const int d = k / 6;
      if (H == 0) { if (d < 3) dma16(dst + d * 1024, L.wvo[d], srd, so); }
      else dma16(dst + d * 1024, L.xvo[d & 1], srd, so + d * 10240);
    }
    SB0();
  }
}

template <bool TR>
DEV void mainloop(f4 (&acc)[4][6], const Loop& L) {
  bf8 x0[4], w0[6], x1[4], w1[6];
  {
#pragma unroll
    for (int i = 0; i < 4; ++i) x0[i] = *(LAS bf8*)(L.xrd[0] + i * 2048);
#pragma unroll
    for (int b = 0; b < 6; ++b) { const u32 a = L.wrd[b % 3] + (b / 3) * 6144; w0[b] = cat8(trread(a), trread(a + 768)); }
  }
  u32 xb = 0, sl = 0;
#pragma unroll 1
  for (int t = 0; t < 10; ++t) {
    const u32 s1 = sl == 4 ? 0 : sl + 1, s2 = s1 == 4 ? 0 : s1 + 1, sm = sl == 0 ? 4 : sl - 1, xn = xb == 2 ? 0 : xb + 1;
    {
      u4 srd = L.wsrd; srd[2] = t < 8 ? L.wnrec : 0u;
      const u32 dst = WRING + (L.whalf ? sl : sm) * WSLOT + L.wdst, so = L.wso + (t + 2) * L.wpitch64;
      const u32 xa = L.xrd[1] + xb * XBUF;
      const u32 wa[3] = {L.wrd[0] + s1 * WSLOT, L.wrd[1] + s1 * WSLOT, L.wrd[2] + s1 * WSLOT};
      SB0(); LGKM0(); BARRIER(); SB0();
      half_step<TR, 0>(acc, x0, w0, x1, w1, xa, wa, L, srd, dst, so);
    }
    {
      u4 srd = L.xsrd; srd[2] = t < 7 ? L.xnrec : 0u;
      const u32 dst = xb * XBUF + L.xdst, so = L.xso + (t + 3) * 128;
      const u32 xa = L.xrd[0] + xn * XBUF;
      const u32 wa[3] = {L.wrd[0] + s2 * WSLOT, L.wrd[1] + s2 * WSLOT, L.wrd[2] + s2 * WSLOT};
      SB0(); VMCNT(7); LGKM0(); BARRIER(); SB0();
      half_step<TR, 1>(acc, x1, w1, x0, w0, xa, wa, L, srd, dst, so);
    }
    xb = xn; sl = s2;
  }
  VMCNT(0); LGKM0();
}

__global__ __launch_bounds__(512, 2) void k1(const u16* __restrict__ x, const u16* __restrict__ wqkv, const u16* __restrict__ wo,
                                             const u16* __restrict__ wg, const u16* __restrict__ wu, const u16* __restrict__ wd,
                                             const u16* __restrict__ gw, char* __restrict__ ws) {
  extern __shared__ __attribute__((aligned(16))) char smem[];
  const int tid = threadIdx.x, lane = tid & 63, w = __builtin_amdgcn_readfirstlane(tid >> 6);
  const int bid = blockIdx.x, xcd = bid & 7, jj = bid >> 3;
  const int g = lane >> 4, r16 = lane & 15;
  STAMP(0, 0);
  if (jj >= 30) {
    const int tw = (jj - 30) * 8 + xcd;
    if (tw == 0 && tid < 256) *(u32*)(ws + FLAG_OFF + tid * 64) = 0u;
    u32 vo[5];
#pragma unroll
    for (int i = 0; i < 5; ++i) { const int n = i * 64 + lane, row = 4 * w + n / 80, p = n % 80; vo[i] = row * 1280 + ((p ^ ((row & 7) << 1)) << 4); }
    const u4 wdsrd = make_srd(wd, F_ * D_ * 2), wosrd = make_srd(wo, D_ * D_ * 2);
    asm volatile("s_nop 4");
#pragma unroll
    for (int s = 0; s < 3; ++s)
#pragma unroll
      for (int i = 0; i < 5; ++i) dma16(s * 40960 + w * 5120 + i * 1024, vo[i], wdsrd, (tw + 16 * s) * 40960);
    {
      const int row = tw * 128 + w * 16 + (lane >> 2);
      const u4* xp = (const u4*)(x + row * D_) + (lane & 3);
      const u4* gp = (const u4*)gw + (lane & 3);
      float a0 = 0.f, a1 = 0.f;
#pragma unroll
      for (int c = 0; c < 20; ++c) {
        const u4 xv = xp[4 * c], gv = gp[4 * c];
#pragma unroll
        for (int e = 0; e < 4; ++e) { a0 += bflo(xv[e]) * bflo(gv[e]); a1 += bfhi(xv[e]) * bfhi(gv[e]); }
      }
      a0 += a1;
      a0 += __shfl_xor(a0, 1); a0 += __shfl_xor(a0, 2);
      if ((lane & 3) == 0) ((float*)(ws + GS_OFF))[row] = sigm(a0);
    }
    const int q = r16 >> 2, p = lane & 3;
    auto tile_wd = [&](u32 slab, int t) {
#pragma unroll
      for (int nb = 0; nb < 5; ++nb) {
        const int ch = 2 * (w * 5 + nb) + (p >> 1), row = 4 * g + q;
        const u32 a = slab + row * 1280 + ((ch ^ ((row & 7) << 1)) << 4) + 8 * (p & 1);
        const bf8 v = cat8(trread(a), trread(a + 16 * 1280));
        *(bf8*)(ws + BF_OFF + ((w * KS2 + t) * 5 + nb) * 1024 + lane * 16) = v;
      }
    };
    VMCNT(0); BARRIER();
#pragma unroll
    for (int s = 0; s < 3; ++s) tile_wd(s * 40960, tw + 16 * s);
    LGKM0(); BARRIER();
    if (tw < 6) {
#pragma unroll
      for (int i = 0; i < 5; ++i) dma16(w * 5120 + i * 1024, vo[i], wdsrd, (tw + 48) * 40960);
    }
    if (w < 5) {
#pragma unroll
      for (int s = 0; s < 2; ++s)
#pragma unroll
        for (int i = 0; i < 5; ++i) dma16(40960 + s * 25600 + w * 5120 + i * 1024, vo[i], wosrd, (tw * 2 + s) * 25600);
    }
    VMCNT(0); BARRIER();
    if (tw < 6) tile_wd(0, tw + 48);
#pragma unroll
    for (int s = 0; s < 2; ++s)
#pragma unroll
      for (int nb = 0; nb < 5; ++nb) {
        const int ch = 2 * (w * 5 + nb) + (p >> 1);
        s4 v[2];
#pragma unroll
        for (int t = 0; t < 2; ++t) {
          const int e0 = 8 * g + 4 * t, row = (e0 < 20 ? e0 : 0) + q;
          v[t] = trread(40960 + s * 25600 + row * 1280 + ((ch ^ ((row & 7) << 1)) << 4) + 8 * (p & 1));
          if (e0 >= 20) v[t] = s4{0, 0, 0, 0};
        }
        *(bf8*)(ws + WOT_OFF + ((tw * 2 + s) * 40 + w * 5 + nb) * 1024 + lane * 16) = cat8(v[0], v[1]);
      }
    STAMP(0, 3);
    return;
  }
  const int idx = (xcd >> 1) * 30 + jj, ct = idx >> 2, rt = (xcd & 1) * 4 + (idx & 3);
  const int type = ct < 18 ? 0 : (ct < 26 ? 1 : 2);
  const int c = type == 0 ? ct : (type == 1 ? ct - 18 : ct - 26);
  const int wm = w >> 1, j = w & 1;
  Loop L;
  {
    const int part = w >> 2, wq = w & 3;
    const u32 pitch = type == 0 ? F_ * 2 : 3 * D_ * 2;
    const u32 colbase = type == 0 ? 96 * c : (type == 1 ? (part ? 1280 : 640) + 80 * c : 192 * c + 96 * part);
    L.wnrec = D_ * pitch; L.xnrec = S_ * D_ * 2;
    L.wsrd = make_srd(type == 0 ? (part ? wu : wg) : wqkv, L.wnrec);
    L.xsrd = make_srd(x, L.xnrec);
    L.wpitch64 = 64 * pitch;
    L.wso = 16 * wq * pitch + colbase * 2;
    L.xso = (rt * 256 + w * 32) * 1280;
#pragma unroll
    for (int i = 0; i < 3; ++i) { const int n = i * 64 + lane, row = n / 12, pos = n % 12; L.wvo[i] = row * pitch + ((pos ^ (((row >> 3) & 1) << 1)) << 4); }
#pragma unroll
    for (int i = 0; i < 2; ++i) L.xvo[i] = (lane >> 3) * 1280 + (((lane & 7) ^ ((4 * i + (lane >> 4)) & 7)) << 4);
    L.whalf = wq >> 1;
    L.wdst = part * 6144 + (wq & 1) * 3072;
    L.xdst = w * 4096;
#pragma unroll
    for (int s = 0; s < 2; ++s) L.xrd[s] = (64 * wm + r16) * 128 + (((4 * s + g) ^ (r16 >> 1)) << 4);
    const int q = r16 >> 2, p = lane & 3;
#pragma unroll
    for (int b = 0; b < 3; ++b) {
      const int ch = type == 1 ? 5 * j + 2 * b : (j ? (b == 2 ? 6 : 8 + 2 * b) : 2 * b);
      L.wrd[b] = WRING + (8 * g + q) * 192 + (((ch + (p >> 1)) ^ ((g & 1) << 1)) << 4) + 8 * (p & 1);
    }
  }
  asm volatile("s_nop 4");
#pragma unroll
  for (int t = 0; t < 3; ++t) {
#pragma unroll
    for (int i = 0; i < 4; ++i) dma16(t * XBUF + L.xdst + i * 1024, L.xvo[i & 1], L.xsrd, L.xso + i * 10240 + t * 128);
    if (t < 2) {
#pragma unroll
      for (int i = 0; i < 3; ++i) dma16(WRING + (2 * t + L.whalf) * WSLOT + L.wdst + i * 1024, L.wvo[i], L.wsrd, L.wso + t * L.wpitch64);
    }
  }
  f4 acc[4][6];
#pragma unroll
  for (int i = 0; i < 4; ++i)
#pragma unroll
    for (int b = 0; b < 6; ++b) acc[i][b] = f4{0.f, 0.f, 0.f, 0.f};
  VMCNT(11); BARRIER();
  STAMP(0, 1); STAMPC(0, 6);
  char* af = ws + AF_OFF + (u32)(rt * 4 + wm) * (KS2 * 4096u) + lane * 16;
  if (type != 1) {
    mainloop<true>(acc, L);
    STAMP(0, 2); STAMPC(0, 7);
    if (type == 0) {
#pragma unroll
      for (int i = 0; i < 4; ++i) {
        u2 hv[3];
#pragma unroll
        for (int b = 0; b < 3; ++b) {
          f4 h;
#pragma unroll
          for (int e = 0; e < 4; ++e) h[e] = sigm(acc[i][b][e]) * acc[i][b + 3][e];
          hv[b] = pk4(h);
        }
        *(u4*)(af + (3 * c + 2 * j) * 4096 + i * 1024) = u4{hv[0][0], hv[0][1], hv[1][0], hv[1][1]};
        *(u2*)(af + (3 * c + 1) * 4096 + i * 1024 + j * 8) = hv[2];
      }
    } else {
#pragma unroll
      for (int part = 0; part < 2; ++part) {
        const int t0 = 6 * c + 3 * part;
#pragma unroll
        for (int i = 0; i < 4; ++i) {
          u2 hv[3];
#pragma unroll
          for (int b = 0; b < 3; ++b) {
            f4 h;
#pragma unroll
            for (int e = 0; e < 4; ++e) h[e] = phi(acc[i][3 * part + b][e]);
            hv[b] = pk4(h);
          }
          if (t0 + 2 * j < 20) *(u4*)(af + (KH + t0 + 2 * j) * 4096 + i * 1024) = u4{hv[0][0], hv[0][1], hv[1][0], hv[1][1]};
          if (t0 + 1 < 20) *(u2*)(af + (KH + t0 + 1) * 4096 + i * 1024 + j * 8) = hv[2];
        }
      }
    }
  } else {
    mainloop<false>(acc, L);
    STAMP(0, 2); STAMPC(0, 7);
    f4 kv[3][3];
#pragma unroll
    for (int a = 0; a < 3; ++a)
#pragma unroll
      for (int b = 0; b < 3; ++b) kv[a][b] = f4{0.f, 0.f, 0.f, 0.f};
#pragma unroll
    for (int s = 0; s < 2; ++s) {
      u4 pk[3], vv[3];
#pragma unroll
      for (int b = 0; b < 3; ++b) {
        f4 p0, p1;
#pragma unroll
        for (int e = 0; e < 4; ++e) { p0[e] = phi(acc[2 * s][b][e]); p1[e] = phi(acc[2 * s + 1][b][e]); }
        const u2 a0 = pk4(p0), a1 = pk4(p1), b0 = pk4(acc[2 * s][3 + b]), b1 = pk4(acc[2 * s + 1][3 + b]);
        pk[b] = u4{a0[0], a0[1], a1[0], a1[1]};
        vv[b] = u4{b0[0], b0[1], b1[0], b1[1]};
      }
      if (r16 == 8) vv[2] = u4{0x3f803f80u, 0x3f803f80u, 0x3f803f80u, 0x3f803f80u};
#pragma unroll
      for (int a = 0; a < 3; ++a)
#pragma unroll
        for (int b = 0; b < 3; ++b) kv[a][b] = mfma(pk[a], vv[b], kv[a][b]);
    }
    BARRIER();
#pragma unroll
    for (int a = 0; a < 3; ++a)
#pragma unroll
      for (int b = 0; b < 3; ++b) *(LAS f4*)((w * 9 + a * 3 + b) * 1024 + lane * 16) = kv[a][b];
    LGKM0(); BARRIER();
#pragma unroll
    for (int it = 0; it < 4; ++it) {
      const int o = it * 512 + tid;
      if (o < 2000) {
        const bool isz = o >= 1920;
        const int hl = isz ? (o - 1920) / 20 : o / 480, d = isz ? (o - 1920) % 20 : (o % 480) / 24, e = isz ? 0 : o % 24;
        const int dj = 20 * (hl & 1) + d, ej = isz ? 40 : 20 * (hl & 1) + e;
        const u32 a = (((hl >> 1) * 9 + (dj >> 4) * 3 + (ej >> 4)) * 64 + ((dj & 15) >> 2) * 16 + (ej & 15)) * 16 + (dj & 3) * 4;
        float sum = 0.f;
#pragma unroll
        for (int m = 0; m < 4; ++m) sum += *(LAS float*)(a + m * 18432);
        if (isz) ((float*)(ws + ZP_OFF))[rt * 640 + c * 80 + (o - 1920)] = sum;
        else ((float*)(ws + KVP_OFF))[(rt * 32 + c * 4) * 480 + o] = e < 20 ? sum : 0.f;
      }
    }
  }
  STAMP(0, 3);
}

constexpr int AUX_SC1 = 16;
constexpr u32 OOB = 0x40000000u;
#define BL128(rs, vo, so, aux) __builtin_amdgcn_raw_buffer_load_b128(rs, vo, so, aux)

__global__ __launch_bounds__(256, 1) void k2(char* __restrict__ ws, u16* __restrict__ out) {
  extern __shared__ __attribute__((aligned(16))) char smem[];
  const int tid = threadIdx.x, lane = tid & 63, w = __builtin_amdgcn_readfirstlane(tid >> 6);
  const int bid = blockIdx.x, xcd = bid & 7, jj = bid >> 3;
  const int rt = (xcd >> 1) * 8 + (jj >> 2), ct = (xcd & 1) * 4 + (jj & 3);
  const int g = lane >> 4, r16 = lane & 15;
  const auto rs = __builtin_amdgcn_make_buffer_rsrc(ws, 0, WS_BYTES, 0x00020000);
  const u32 vo = lane * 16;
  const u32 abase = AF_OFF + rt * (KS2 * 4096u), bbase = BF_OFF + ct * (KS2 * 5120u);
  STAMP(1, 0);
  if (w == 0) {
    u4 pv[8][2][2], wt[5];
    const u32 o0 = g < 3 ? r16 * 96 + g * 32 : OOB, o1 = (g < 3 && r16 < 4) ? (16 + r16) * 96 + g * 32 : OOB;
#pragma unroll
    for (int r = 0; r < 8; ++r) {
      const u32 so = KVP_OFF + (r * 32 + rt) * 1920;
      pv[r][0][0] = BL128(rs, o0, so, 0); pv[r][0][1] = BL128(rs, o0 + 16, so, 0);
      pv[r][1][0] = BL128(rs, o1, so, 0); pv[r][1][1] = BL128(rs, o1 + 16, so, 0);
    }
#pragma unroll
    for (int nb = 0; nb < 5; ++nb) wt[nb] = BL128(rs, vo, WOT_OFF + (rt * 40 + ct * 5 + nb) * 1024, 0);
#pragma unroll
    for (int blk = 0; blk < 2; ++blk) {
      f4 s0 = __builtin_bit_cast(f4, pv[0][blk][0]), s1 = __builtin_bit_cast(f4, pv[0][blk][1]);
#pragma unroll
      for (int r = 1; r < 8; ++r) { s0 += __builtin_bit_cast(f4, pv[r][blk][0]); s1 += __builtin_bit_cast(f4, pv[r][blk][1]); }
      const u2 a0 = pk4(s0), a1 = pk4(s1);
      const u4 a = {a0[0], a0[1], a1[0], a1[1]};
      const int q = 5 * rt + 4 * blk + g;
      const u32 dst = (blk == 0 || g == 0) ? (KH + (q >> 3)) * 5120 + ((q & 3) * 16 + r16) * 16 + ((q >> 2) & 1) * 8 : OOB;
#pragma unroll
      for (int nb = 0; nb < 5; ++nb) {
        const f4 m = mfma(a, wt[nb], f4{0.f, 0.f, 0.f, 0.f});
        __builtin_amdgcn_raw_buffer_store_b64(pk4(m), rs, dst, bbase + nb * 1024, AUX_SC1);
      }
    }
    VMCNT(0);
    if (lane == 0) __hip_atomic_store((GAS u32*)(ws + FLAG_OFF + (rt * 8 + ct) * 64), 1u, __ATOMIC_RELAXED, __HIP_MEMORY_SCOPE_AGENT);
    SB0();
  }
  STAMP(1, 1);
  u4 zp[8], qf[5][4];
  float gs[4];
  {
    const u32 zo = (lane < 40 ? lane : 39) * 16;
#pragma unroll
    for (int r = 0; r < 8; ++r) zp[r] = BL128(rs, zo, ZP_OFF + r * 2560 + w * 640, 0);
#pragma unroll
    for (int i = 0; i < 4; ++i) gs[i] = __builtin_bit_cast(float, __builtin_amdgcn_raw_buffer_load_b32(rs, r16 * 4 + i * 64, GS_OFF + rt * 256, 0));
#pragma unroll
    for (int t = 0; t < 5; ++t)
#pragma unroll
      for (int i = 0; i < 4; ++i) qf[t][i] = BL128(rs, vo + i * 1024, abase + (KH + 5 * w + t) * 4096, 0);
  }
  constexpr int NST = 14, DEP = 3;
  const int t0 = w == 0 ? 0 : 14 * w - 2;
  u4 fa[DEP + 1][4], fb[DEP + 1][5];
  auto ld = [&](int s) {
    const bool off = (s >= 12) && (w == 0);
    const u32 sa = off ? OOB : abase + (t0 + s) * 4096, sb = off ? OOB : bbase + (t0 + s) * 5120;
#pragma unroll
    for (int i = 0; i < 4; ++i) fa[s % (DEP + 1)][i] = BL128(rs, vo + i * 1024, sa, 0);
#pragma unroll
    for (int n = 0; n < 5; ++n) fb[s % (DEP + 1)][n] = BL128(rs, vo + (n & 3) * 1024, sb + (n >> 2) * 4096, 0);
  };
#pragma unroll
  for (int s = 0; s < DEP; ++s) ld(s);
  SB0();
  {
    const u32 zl = 81920 + w * 2560, sl = zl + 512;
    f4 z = __builtin_bit_cast(f4, zp[0]);
#pragma unroll
    for (int r = 1; r < 8; ++r) z += __builtin_bit_cast(f4, zp[r]);
    *(LAS u2*)(zl + lane * 8) = pk4(z);
    f4 den[4];
#pragma unroll
    for (int i = 0; i < 4; ++i) den[i] = f4{0.f, 0.f, 0.f, 0.f};
#pragma unroll
    for (int t = 0; t < 5; ++t) {
      const int q0 = 8 * t + g, q1 = q0 + 4;
      u2 lo = *(LAS u2*)(zl + q0 * 8), hi = *(LAS u2*)(zl + q1 * 8);
      if (q0 / 5 != r16) lo = u2{0u, 0u};
      if (q1 / 5 != r16) hi = u2{0u, 0u};
      const u4 zf = {lo[0], lo[1], hi[0], hi[1]};
#pragma unroll
      for (int i = 0; i < 4; ++i) den[i] = mfma(zf, qf[t][i], den[i]);
    }
#pragma unroll
    for (int i = 0; i < 4; ++i) {
      f4 sc;
#pragma unroll
      for (int e = 0; e < 4; ++e) sc[e] = gs[i] * __builtin_amdgcn_rcpf(den[i][e] + 1e-6f);
      if (g < 2) *(LAS f4*)(sl + (i * 16 + r16) * 32 + g * 16) = sc;
    }
#pragma unroll
    for (int t = 0; t < 5; ++t) {
      const int h0 = (8 * t + g) / 5, h1 = (8 * t + 4 + g) / 5;
#pragma unroll
      for (int i = 0; i < 4; ++i) {
        const float s0 = *(LAS float*)(sl + (i * 16 + r16) * 32 + h0 * 4), s1 = *(LAS float*)(sl + (i * 16 + r16) * 32 + h1 * 4);
        const u4 v = qf[t][i];
        qf[t][i] = u4{pk2(bflo(v[0]) * s0, bfhi(v[0]) * s0), pk2(bflo(v[1]) * s0, bfhi(v[1]) * s0),
                      pk2(bflo(v[2]) * s1, bfhi(v[2]) * s1), pk2(bflo(v[3]) * s1, bfhi(v[3]) * s1)};
      }
    }
  }
  SB0();
  STAMP(1, 2);
  f4 acc[4][5];
#pragma unroll
  for (int i = 0; i < 4; ++i)
#pragma unroll
    for (int n = 0; n < 5; ++n) acc[i][n] = f4{0.f, 0.f, 0.f, 0.f};
  u4 mf[5][5];
  GAS u32* flag = (GAS u32*)(ws + FLAG_OFF + ((8 * w + (lane & 7)) * 8 + ct) * 64);
  u32 fl = 0;
#pragma unroll
  for (int s = 0; s < NST; ++s) {
    if (s + DEP < NST) ld(s + DEP);
    if (s + DEP == NST - 2) fl = __hip_atomic_load(flag, __ATOMIC_RELAXED, __HIP_MEMORY_SCOPE_AGENT);
    if (s + DEP == NST) {
      for (u32 spin = 0; !__all(fl == 1u) && spin < (1u << 22); ++spin) {
        __builtin_amdgcn_s_sleep(1);
        fl = __hip_atomic_load(flag, __ATOMIC_RELAXED, __HIP_MEMORY_SCOPE_AGENT);
      }
      __builtin_amdgcn_fence(__ATOMIC_ACQUIRE, "wavefront");
#pragma unroll
      for (int t = 0; t < 5; ++t)
#pragma unroll
        for (int n = 0; n < 5; ++n) mf[t][n] = BL128(rs, vo + (n & 3) * 1024, bbase + (KH + 5 * w + t) * 5120 + (n >> 2) * 4096, AUX_SC1);
    }
    SB0();
#pragma unroll
    for (int n = 0; n < 5; ++n)
#pragma unroll
      for (int i = 0; i < 4; ++i) acc[i][n] = mfma(fb[s % (DEP + 1)][n], fa[s % (DEP + 1)][i], acc[i][n]);
    SB0();
  }
  STAMP(1, 3);
#pragma unroll
  for (int i = 0; i < 4; ++i) {
    const float f = 1.f - gs[i];
#pragma unroll
    for (int n = 0; n < 5; ++n) acc[i][n] *= f;
  }
#pragma unroll
  for (int t = 0; t < 5; ++t)
#pragma unroll
    for (int n = 0; n < 5; ++n)
#pragma unroll
      for (int i = 0; i < 4; ++i) acc[i][n] = mfma(mf[t][n], qf[t][i], acc[i][n]);
  STAMP(1, 4);
#pragma unroll
  for (int i = 0; i < 4; ++i)
#pragma unroll
    for (int n = 0; n < 5; ++n) *(LAS f4*)(((w * 4 + i) * 5 + n) * 1024 + lane * 16) = acc[i][n];
  LGKM0(); BARRIER();
  u16* op = out + (rt * 64 + w * 16 + r16) * D_ + ct * 80 + 4 * g;
#pragma unroll
  for (int n = 0; n < 5; ++n) {
    f4 v = *(LAS f4*)((w * 5 + n) * 1024 + lane * 16);
#pragma unroll
    for (int s = 1; s < 4; ++s) v += *(LAS f4*)(((s * 4 + w) * 5 + n) * 1024 + lane * 16);
    *(u2*)(op + n * 16) = pk4(v);
  }
  STAMP(1, 5);
}

extern "C" void kernel_launch(void* const* d_in, const int* in_sizes, int n_in, void* d_out, int out_size, void* d_ws, size_t ws_size,
                              hipStream_t stream) {
  (void)in_sizes; (void)n_in; (void)out_size; (void)ws_size;
  (void)hipFuncSetAttribute((const void*)k1, hipFuncAttributeMaxDynamicSharedMemorySize, K1_LDS);
  (void)hipFuncSetAttribute((const void*)k2, hipFuncAttributeMaxDynamicSharedMemorySize, K2_LDS);
  k1<<<256, 512, K1_LDS, stream>>>((const u16*)d_in[0], (const u16*)d_in[1], (const u16*)d_in[2], (const u16*)d_in[4], (const u16*)d_in[5],
                                   (const u16*)d_in[6], (const u16*)d_in[7], (char*)d_ws);
  k2<<<256, 256, K2_LDS, stream>>>((char*)d_ws, (u16*)d_out);
}
```

```cpp
#include <hip/hip_runtime.h>
#include <stdint.h>

typedef unsigned short u16;
typedef unsigned int u32;
typedef unsigned long long u64;
typedef short s4 __attribute__((ext_vector_type(4)));
typedef short s8 __attribute__((ext_vector_type(8)));
typedef __bf16 bf8 __attribute__((ext_vector_type(8)));
typedef __bf16 bf2 __attribute__((ext_vector_type(2)));
typedef float f2 __attribute__((ext_vector_type(2)));
typedef float f4 __attribute__((ext_vector_type(4)));
typedef u32 u4 __attribute__((ext_vector_type(4)));
typedef u32 u2 __attribute__((ext_vector_type(2)));
#define LAS __attribute__((address_space(3)))
#define GAS __attribute__((address_space(1)))
#define DEV __device__ __forceinline__
#define SB0() __builtin_amdgcn_sched_barrier(0)

constexpr int S_ = 2048, D_ = 640, F_ = 1728;
constexpr int KS2 = 74, KH = 54;
constexpr u32 AF_OFF = 0, AF_BYTES = 32u * KS2 * 4096u;
constexpr u32 BF_OFF = AF_OFF + AF_BYTES, BF_BYTES = 8u * KS2 * 5120u;
constexpr u32 WOT_OFF = BF_OFF + BF_BYTES, WOT_BYTES = 32u * 40u * 1024u;
constexpr u32 KVP_OFF = WOT_OFF + WOT_BYTES, KVP_BYTES = 8u * 32u * 400u * 4u;
constexpr u32 ZP_OFF = KVP_OFF + KVP_BYTES, ZP_BYTES = 8u * 640u * 4u;
constexpr u32 GS_OFF = ZP_OFF + ZP_BYTES, GS_BYTES = 2048u * 4u;
constexpr u32 FLAG_OFF = GS_OFF + GS_BYTES, FLAG_BYTES = 256u * 64u;
constexpr u32 WS_BYTES = FLAG_OFF + FLAG_BYTES;
#define STAMP(k, i) do {} while (0)
#define STAMPC(k, i) do {} while (0)

constexpr u32 XBUF = 32768, WRING = 3 * XBUF, WSLOT = 12288, K1_LDS = WRING + 5 * WSLOT;
constexpr u32 K2_LDS = 163840;
constexpr u32 OOB = 0x40000000u;

DEV u4 make_srd(const void* p, u32 bytes) {
  u64 a = (u64)p;
  u4 r = {(u32)a, (u32)(a >> 32) & 0xffffu, bytes, 0x00020000u};
  return r;
}
DEV void dma16(u32 lds, u32 voff, u4 srd, u32 soff) {
  asm volatile("s_mov_b32 m0, %0\n\ts_nop 0\n\tbuffer_load_dwordx4 %1, %2, %3 offen lds" ::"s"(lds), "v"(voff), "s"(srd), "s"(soff) : "memory");
}
DEV s4 trread(u32 a) { return __builtin_amdgcn_ds_read_tr16_b64_v4i16((LAS s4*)a); }
DEV bf8 cat8(s4 lo, s4 hi) { return __builtin_bit_cast(bf8, __builtin_shufflevector(lo, hi, 0, 1, 2, 3, 4, 5, 6, 7)); }
DEV f4 mfma(bf8 a, bf8 b, f4 c) { return __builtin_amdgcn_mfma_f32_16x16x32_bf16(a, b, c, 0, 0, 0); }
DEV f4 mfma(u4 a, u4 b, f4 c) { return mfma(__builtin_bit_cast(bf8, a), __builtin_bit_cast(bf8, b), c); }
DEV u32 pk2(float a, float b) { f2 v = {a, b}; return __builtin_bit_cast(u32, __builtin_convertvector(v, bf2)); }
DEV u2 pk4(f4 v) { u2 r = {pk2(v[0], v[1]), pk2(v[2], v[3])}; return r; }
DEV float bflo(u32 v) { return __builtin_bit_cast(float, v << 16); }
DEV float bfhi(u32 v) { return __builtin_bit_cast(float, v & 0xffff0000u); }
DEV float sigm(float x) { return __builtin_amdgcn_rcpf(1.f + __builtin_amdgcn_exp2f(-1.44269504089f * x)); }
DEV float phi(float t) {
  return __builtin_fmaxf(t + 1.f, __builtin_amdgcn_fmed3f(__builtin_amdgcn_exp2f(1.44269504089f * t), 0.f, 1.f)) + 1e-6f;
}
#define LGKM0() __builtin_amdgcn_s_waitcnt(0xC07F)
#define VMCNT(n) asm volatile("s_waitcnt vmcnt(" #n ")" ::: "memory")
#define BARRIER() do { asm volatile("" ::: "memory"); __builtin_amdgcn_s_barrier(); asm volatile("" ::: "memory"); } while (0)

struct Loop {
  u4 xsrd, wsrd;
  u32 xnrec, wnrec, xso, wso, wpitch64;
  u32 xvo[2], wvo[3];
  u32 xdst, wdst, whalf, young;
  u32 xrd[2], wrd[3];
};
struct St { u32 xa, wa[3], rw, rx, dw, dx, sw, sx, nw, nx; };
DEV u32 wrapw(u32 v) { return v >= 5 * WSLOT ? v - 5 * WSLOT : v; }

#define RPAT 1
#define DPAT 1
#define PRIO 0
constexpr int rslot(int r) {
  return RPAT == 0 ? (r < 4 ? r : 4 + (r - 4) / 2) : RPAT == 1 ? r : RPAT == 2 ? (r * 4) / 3 : RPAT == 3 ? -1 : RPAT == 4 ? (r * 5) / 4 : r + 4;
}
template <bool TR, int H>
DEV void half_step(f4 (&acc)[4][6], const bf8 (&xc)[4], const bf8 (&wc)[6], bf8 (&xn)[4], bf8 (&wn)[6], const Loop& L, St& s, int t) {
  const u32 xa = s.xa, wa[3] = {s.wa[0], s.wa[1], s.wa[2]};
  u4 srd = H == 0 ? L.wsrd : L.xsrd;
  srd[2] = H == 0 ? s.nw : s.nx;
  const u32 dst = H == 0 ? s.dw : s.dx, so = H == 0 ? s.sw : s.sx;
  auto dma1 = [&](int d) {
    if (H == 0) { if (d < 3) dma16(dst + d * 1024, L.wvo[d], srd, so); }
    else dma16(dst + d * 1024, L.xvo[d & 1], srd, so + d * 10240);
  };
  auto dma = [&]() {
#pragma unroll
    for (int d = 0; d < 4; ++d) dma1(d);
  };
  s4 lo[6];
  auto rd = [&](int r) {
    if (r < 4) xn[r] = *(LAS bf8*)(xa + r * 2048);
    else { const int nb = (r - 4) >> 1; const u32 a = wa[nb % 3] + (nb / 3) * 6144;
      if (r & 1) wn[nb] = cat8(lo[nb], trread(a + 768)); else lo[nb] = trread(a); }
  };
  if (DPAT == 0) { if (L.young) dma(); }
  if (DPAT == 3) dma();
  SB0();
#pragma unroll
  for (int r = 0; r < 16; ++r) if (rslot(r) < 0) rd(r);
  SB0();
#pragma unroll
  for (int k = 0; k < 24; ++k) {
    const int b = k >> 2, i = k & 3;
    acc[i][b] = TR ? mfma(wc[b], xc[i], acc[i][b]) : mfma(xc[i], wc[b], acc[i][b]);
    SB0();
#pragma unroll
    for (int r = 0; r < 16; ++r) if (rslot(r) == k) rd(r);
    if (DPAT == 1 && k % 6 == 1) dma1(k / 6);
    if (DPAT == 2 && k % 3 == 1 && k < 12) dma1(k / 3);
    if (DPAT == 5 && k % 3 == 1) { if (k < 12) { if (L.young) dma1(k / 3); } else { if (!L.young) dma1(k / 3 - 4); } }
    if (k == 21) {
      asm volatile("" : "+s"(s.rw), "+s"(s.rx));
      s.rw = wrapw(s.rw + WSLOT);
      if (H == 0) s.rx = s.rx == 2 * XBUF ? 0 : s.rx + XBUF;
      s.xa = L.xrd[H] + s.rx;
#pragma unroll
      for (int b2 = 0; b2 < 3; ++b2) s.wa[b2] = L.wrd[b2] + s.rw;
      asm volatile("" : "+v"(s.xa), "+v"(s.wa[0]), "+v"(s.wa[1]), "+v"(s.wa[2]));
    } else if (k == 11) {
      if (H == 1) {
        asm volatile("" : "+s"(s.dw), "+s"(s.sw), "+s"(t));
        s.dw = WRING + wrapw(s.dw - WRING + 2 * WSLOT); s.sw += L.wpitch64; s.nw = t < 7 ? L.wnrec : 0u;
        asm volatile("" : "+s"(s.dw), "+s"(s.sw), "+s"(s.nw));
      } else {
        asm volatile("" : "+s"(s.dx), "+s"(s.sx), "+s"(t));
        s.dx = s.dx >= 2 * XBUF ? s.dx - 2 * XBUF : s.dx + XBUF; s.sx += 128; s.nx = t < 7 ? L.xnrec : 0u;
        asm volatile("" : "+s"(s.dx), "+s"(s.sx), "+s"(s.nx));
      }
    }
    SB0();
  }
  if (DPAT == 0) { if (!L.young) dma(); }
  if (DPAT == 4) dma();
}

template <bool TR>
DEV void mainloop(f4 (&acc)[4][6], const Loop& L) {
  bf8 x0[4], w0[6], x1[4], w1[6];
  {
#pragma unroll
    for (int i = 0; i < 4; ++i) x0[i] = *(LAS bf8*)(L.xrd[0] + i * 2048);
#pragma unroll
    for (int b = 0; b < 6; ++b) { const u32 a = L.wrd[b % 3] + (b / 3) * 6144; w0[b] = cat8(trread(a), trread(a + 768)); }
  }
  St s;
  s.rw = WSLOT; s.rx = 0; s.xa = L.xrd[1];
#pragma unroll
  for (int b = 0; b < 3; ++b) s.wa[b] = L.wrd[b] + WSLOT;
  s.dw = WRING + (L.whalf ? 0 : 4 * WSLOT) + L.wdst; s.sw = L.wso + 2 * L.wpitch64; s.nw = L.wnrec;
  s.dx = 2 * XBUF + L.xdst; s.sx = L.xso + 2 * 128; s.nx = L.xnrec;
#pragma unroll 1
  for (int t = 0; t < 10; ++t) {
    SB0(); LGKM0(); BARRIER(); SB0();
    half_step<TR, 0>(acc, x0, w0, x1, w1, L, s, t);
    SB0(); VMCNT(7); LGKM0(); BARRIER(); SB0();
    half_step<TR, 1>(acc, x1, w1, x0, w0, L, s, t);
  }
  __builtin_amdgcn_s_setprio(0);
  VMCNT(0); LGKM0();
}

__global__ __launch_bounds__(512, 2) void k1(const u16* __restrict__ x, const u16* __restrict__ wqkv, const u16* __restrict__ wo,
                                             const u16* __restrict__ wg, const u16* __restrict__ wu, const u16* __restrict__ wd,
                                             const u16* __restrict__ gw, char* __restrict__ ws) {
  extern __shared__ __attribute__((aligned(16))) char smem[];
  const int tid = threadIdx.x, lane = tid & 63, w = __builtin_amdgcn_readfirstlane(tid >> 6);
  const int bid = blockIdx.x, xcd = bid & 7, jj = bid >> 3;
  const int g = lane >> 4, r16 = lane & 15;
  STAMP(0, 0);
  if (jj >= 30) {
    const int tw = (jj - 30) * 8 + xcd;
    if (tw == 0 && tid < 256) *(u32*)(ws + FLAG_OFF + tid * 64) = 0u;
    u32 vo[5];
#pragma unroll
    for (int i = 0; i < 5; ++i) { const int n = i * 64 + lane, row = 4 * w + n / 80, p = n % 80; vo[i] = row * 1280 + ((p ^ ((row & 7) << 1)) << 4); }
    const u4 wdsrd = make_srd(wd, F_ * D_ * 2), wosrd = make_srd(wo, D_ * D_ * 2);
    asm volatile("s_nop 4");
#pragma unroll
    for (int s = 0; s < 3; ++s)
#pragma unroll
      for (int i = 0; i < 5; ++i) dma16(s * 40960 + w * 5120 + i * 1024, vo[i], wdsrd, (tw + 16 * s) * 40960);
    {
      const int row = tw * 128 + w * 16 + (lane >> 2);
      const u4* xp = (const u4*)(x + row * D_) + (lane & 3);
      const u4* gp = (const u4*)gw + (lane & 3);
      float a0 = 0.f, a1 = 0.f;
#pragma unroll
      for (int c = 0; c < 20; ++c) {
        const u4 xv = xp[4 * c], gv = gp[4 * c];
#pragma unroll
        for (int e = 0; e < 4; ++e) { a0 += bflo(xv[e]) * bflo(gv[e]); a1 += bfhi(xv[e]) * bfhi(gv[e]); }
      }
      a0 += a1;
      a0 += __shfl_xor(a0, 1); a0 += __shfl_xor(a0, 2);
      if ((lane & 3) == 0) ((float*)(ws + GS_OFF))[row] = sigm(a0);
    }
    const int q = r16 >> 2, p = lane & 3;
    auto tile_wd = [&](u32 slab, int t) {
#pragma unroll
      for (int nb = 0; nb < 5; ++nb) {
        const int ch = 2 * (w * 5 + nb) + (p >> 1), row = 4 * g + q;
        const u32 a = slab + row * 1280 + ((ch ^ ((row & 7) << 1)) << 4) + 8 * (p & 1);
        const bf8 v = cat8(trread(a), trread(a + 16 * 1280));
        *(bf8*)(ws + BF_OFF + ((w * KS2 + t) * 5 + nb) * 1024 + lane * 16) = v;
      }
    };
    VMCNT(0); BARRIER();
#pragma unroll
    for (int s = 0; s < 3; ++s) tile_wd(s * 40960, tw + 16 * s);
    LGKM0(); BARRIER();
    if (tw < 6) {
#pragma unroll
      for (int i = 0; i < 5; ++i) dma16(w * 5120 + i * 1024, vo[i], wdsrd, (tw + 48) * 40960);
    }
    if (w < 5) {
#pragma unroll
      for (int s = 0; s < 2; ++s)
#pragma unroll
        for (int i = 0; i < 5; ++i) dma16(40960 + s * 25600 + w * 5120 + i * 1024, vo[i], wosrd, (tw * 2 + s) * 25600);
    }
    VMCNT(0); BARRIER();
    if (tw < 6) tile_wd(0, tw + 48);
#pragma unroll
    for (int s = 0; s < 2; ++s)
#pragma unroll
      for (int nb = 0; nb < 5; ++nb) {
        const int ch = 2 * (w * 5 + nb) + (p >> 1);
        s4 v[2];
#pragma unroll
        for (int t = 0; t < 2; ++t) {
          const int e0 = 8 * g + 4 * t, row = (e0 < 20 ? e0 : 0) + q;
          v[t] = trread(40960 + s * 25600 + row * 1280 + ((ch ^ ((row & 7) << 1)) << 4) + 8 * (p & 1));
          if (e0 >= 20) v[t] = s4{0, 0, 0, 0};
        }
        *(bf8*)(ws + WOT_OFF + ((tw * 2 + s) * 40 + w * 5 + nb) * 1024 + lane * 16) = cat8(v[0], v[1]);
      }
    STAMP(0, 3);
    return;
  }
  const int idx = (xcd >> 1) * 30 + jj, rt = (xcd & 1) * 4 + (idx & 3);
  Loop L;
  L.xnrec = S_ * D_ * 2;
  L.xsrd = make_srd(x, L.xnrec);
  L.xso = (rt * 256 + w * 32) * 1280;
#pragma unroll
  for (int i = 0; i < 2; ++i) L.xvo[i] = (lane >> 3) * 1280 + (((lane & 7) ^ ((4 * i + (lane >> 4)) & 7)) << 4);
  L.xdst = w * 4096;
  asm volatile("s_nop 4");
#pragma unroll
  for (int i = 0; i < 4; ++i) dma16(L.xdst + i * 1024, L.xvo[i & 1], L.xsrd, L.xso + i * 10240);
  SB0();
  const int cq = idx >> 2, c15 = cq >= 15 ? cq - 15 : cq, up = cq >= 15;
  const int r8 = c15 & 7, h8 = c15 >> 3;
  const int type = (r8 == 2 || r8 == 5) ? 1 : (r8 == 6 ? 2 : 0);
  const int c = type == 0 ? up * 9 + h8 * 5 + (r8 - (r8 > 2) - (r8 > 5) - (r8 > 6)) : (type == 1 ? up * 4 + h8 * 2 + (r8 == 5) : up * 2 + h8);
  const int wm = w >> 1, j = w & 1;
  {
    const int part = w >> 2, wq = w & 3;
    const u32 pitch = type == 0 ? F_ * 2 : 3 * D_ * 2;
    const u32 colbase = type == 0 ? 96 * c : (type == 1 ? (part ? 1280 : 640) + 80 * c : 192 * c + 96 * part);
    const int nch = type == 0 ? 12 : (type == 1 ? 10 : (c < 3 ? 12 : (part ? 0 : 8)));
    L.wnrec = D_ * pitch;
    L.wsrd = make_srd(type == 0 ? (part ? wu : wg) : wqkv, L.wnrec);
    L.wpitch64 = 64 * pitch;
    L.wso = 16 * wq * pitch + colbase * 2;
#pragma unroll
    for (int i = 0; i < 3; ++i) {
      const int n = i * 64 + lane, row = n / 12, ch = (n % 12) ^ (((row >> 3) & 1) << 1);
      L.wvo[i] = ch < nch ? row * pitch + (ch << 4) : OOB;
    }
    L.whalf = wq >> 1;
    L.wdst = part * 6144 + (wq & 1) * 3072;
    L.young = part;
  }
  asm volatile("s_nop 4");
#pragma unroll
  for (int i = 0; i < 3; ++i) dma16(WRING + L.whalf * WSLOT + L.wdst + i * 1024, L.wvo[i], L.wsrd, L.wso);
  BARRIER();
#pragma unroll
  for (int i = 0; i < 4; ++i) dma16(XBUF + L.xdst + i * 1024, L.xvo[i & 1], L.xsrd, L.xso + i * 10240 + 128);
#pragma unroll
  for (int i = 0; i < 3; ++i) dma16(WRING + (2 + L.whalf) * WSLOT + L.wdst + i * 1024, L.wvo[i], L.wsrd, L.wso + L.wpitch64);
#pragma unroll
  for (int i = 0; i < 4; ++i) dma16(2 * XBUF + L.xdst + i * 1024, L.xvo[i & 1], L.xsrd, L.xso + i * 10240 + 256);
  SB0();
  {
#pragma unroll
    for (int s = 0; s < 2; ++s) L.xrd[s] = (64 * wm + r16) * 128 + (((4 * s + g) ^ (r16 >> 1)) << 4);
    const int q = r16 >> 2, p = lane & 3;
#pragma unroll
    for (int b = 0; b < 3; ++b) {
      const int ch = (type == 1 ? 5 : 6) * j + 2 * b;
      L.wrd[b] = WRING + (8 * g + q) * 192 + (((ch + (p >> 1)) ^ ((g & 1) << 1)) << 4) + 8 * (p & 1);
    }
  }
  f4 acc[4][6];
#pragma unroll
  for (int i = 0; i < 4; ++i)
#pragma unroll
    for (int b = 0; b < 6; ++b) acc[i][b] = f4{0.f, 0.f, 0.f, 0.f};
  VMCNT(11); BARRIER();
  STAMP(0, 1); STAMPC(0, 6);
  char* af = ws + AF_OFF + (u32)(rt * 4 + wm) * (KS2 * 4096u) + lane * 16;
  if (type != 1) {
    mainloop<true>(acc, L);
    STAMP(0, 2); STAMPC(0, 7);
    if (type == 0) {
#pragma unroll
      for (int b = 0; b < 3; ++b)
#pragma unroll
        for (int ip = 0; ip < 2; ++ip) {
          f4 h0, h1;
#pragma unroll
          for (int e = 0; e < 4; ++e) {
            h0[e] = sigm(acc[2 * ip][b][e]) * acc[2 * ip][b + 3][e];
            h1[e] = sigm(acc[2 * ip + 1][b][e]) * acc[2 * ip + 1][b + 3][e];
          }
          const u2 v0 = pk4(h0), v1 = pk4(h1);
          *(u4*)(af + (6 * c + 3 * j + b) * 2048 + ip * 1024) = u4{v0[0], v0[1], v1[0], v1[1]};
        }
    } else {
#pragma unroll
      for (int b = 0; b < 6; ++b) {
        const int blk = 12 * c + 6 * (b / 3) + 3 * j + b % 3;
        if (blk < 40) {
#pragma unroll
          for (int ip = 0; ip < 2; ++ip) {
            f4 h0, h1;
#pragma unroll
            for (int e = 0; e < 4; ++e) { h0[e] = phi(acc[2 * ip][b][e]); h1[e] = phi(acc[2 * ip + 1][b][e]); }
            const u2 v0 = pk4(h0), v1 = pk4(h1);
            *(u4*)(af + (2 * KH + blk) * 2048 + ip * 1024) = u4{v0[0], v0[1], v1[0], v1[1]};
          }
        }
      }
    }
  } else {
    mainloop<false>(acc, L);
    STAMP(0, 2); STAMPC(0, 7);
    constexpr int PA[8] = {0, 0, 1, 1, 1, 2, 2, 0}, PB[8] = {0, 1, 0, 1, 2, 1, 2, 2};
    f4 kv[8];
#pragma unroll
    for (int n = 0; n < 8; ++n) kv[n] = f4{0.f, 0.f, 0.f, 0.f};
#pragma unroll
    for (int s = 0; s < 2; ++s) {
      u4 pk[3], vv[3];
#pragma unroll
      for (int b = 0; b < 3; ++b) {
        f4 p0, p1;
#pragma unroll
        for (int e = 0; e < 4; ++e) { p0[e] = phi(acc[2 * s][b][e]); p1[e] = phi(acc[2 * s + 1][b][e]); }
        const u2 a0 = pk4(p0), a1 = pk4(p1), b0 = pk4(acc[2 * s][3 + b]), b1 = pk4(acc[2 * s + 1][3 + b]);
        pk[b] = u4{a0[0], a0[1], a1[0], a1[1]};
        vv[b] = u4{b0[0], b0[1], b1[0], b1[1]};
      }
      if (r16 == 8) vv[2] = u4{0x3f803f80u, 0x3f803f80u, 0x3f803f80u, 0x3f803f80u};
#pragma unroll
      for (int n = 0; n < 8; ++n) kv[n] = mfma(vv[PB[n]], pk[PA[n]], kv[n]);
    }
    BARRIER();
#pragma unroll
    for (int n = 0; n < 8; ++n) *(LAS f4*)((w * 8 + n) * 1024 + lane * 16) = kv[n];
    LGKM0(); BARRIER();
    const int pa = (0x02211100 >> (4 * w)) & 15, pb = (0x22121010 >> (4 * w)) & 15;
    const int dj = 16 * pa + r16, ej = 16 * pb + 4 * g, hh = dj >= 20;
#pragma unroll
    for (int j2 = 0; j2 < 2; ++j2) {
      f4 sum = *(LAS f4*)((j2 * 8 + w) * 1024 + lane * 16);
#pragma unroll
      for (int m = 1; m < 4; ++m) sum += *(LAS f4*)(((2 * m + j2) * 8 + w) * 1024 + lane * 16);
      const int head = 4 * c + 2 * j2 + hh;
      if (dj < 40) {
        if (ej < 40 && (ej >= 20) == hh) *(f4*)(ws + KVP_OFF + ((rt * 32 + head) * 400 + (dj - 20 * hh) * 20 + (ej - 20 * hh)) * 4) = sum;
        if (ej == 40) ((float*)(ws + ZP_OFF))[rt * 640 + c * 80 + 40 * j2 + dj] = sum[0];
      }
    }
  }
  STAMP(0, 3);
}

constexpr int AUX_SC1 = 16;
#define BL128(rs, vo, so, aux) __builtin_amdgcn_raw_buffer_load_b128(rs, vo, so, aux)
struct Stage { u4 a[4], b[5]; };
DEV void unpair(const u4 (&p)[4], u4 (&f)[4]) {
#pragma unroll
  for (int ip = 0; ip < 2; ++ip) {
    f[2 * ip] = u4{p[ip][0], p[ip][1], p[2 + ip][0], p[2 + ip][1]};
    f[2 * ip + 1] = u4{p[ip][2], p[ip][3], p[2 + ip][2], p[2 + ip][3]};
  }
}
DEV void step_mma(f4 (&acc)[4][5], const u4 (&a)[4], const u4 (&b)[5]) {
#pragma unroll
  for (int n = 0; n < 5; ++n)
#pragma unroll
    for (int i = 0; i < 4; ++i) acc[i][n] = mfma(b[n], a[i], acc[i][n]);
}
constexpr int NH = 11, NO = 3;

__global__ __launch_bounds__(512, 2) void k2(char* __restrict__ ws, u16* __restrict__ out) {
  extern __shared__ __attribute__((aligned(16))) char smem[];
  const int tid = threadIdx.x, lane = tid & 63, w = __builtin_amdgcn_readfirstlane(tid >> 6);
  const int bid = blockIdx.x, xcd = bid & 7, jj = bid >> 3;
  const int rt = (xcd >> 1) * 8 + (jj >> 2), ct = (xcd & 1) * 4 + (jj & 3);
  const int g = lane >> 4, r16 = lane & 15;
  const auto rs = __builtin_amdgcn_make_buffer_rsrc(ws, 0, WS_BYTES, 0x00020000);
  const u32 vo = lane * 16;
  const u32 abase = AF_OFF + rt * (KS2 * 4096u), bbase = BF_OFF + ct * (KS2 * 5120u);
  STAMP(1, 0);
  auto ld = [&](Stage& st, u32 sa, u32 sb) {
#pragma unroll
    for (int i = 0; i < 4; ++i) st.a[i] = BL128(rs, vo + i * 1024, sa, 0);
#pragma unroll
    for (int n = 0; n < 5; ++n) st.b[n] = BL128(rs, vo + (n & 3) * 1024, sb + (n >> 2) * 4096, 0);
  };
  f4 acc[4][5];
  float gs[4];
  if (w < 4) {
    constexpr int DEP = 3;
    Stage st[DEP + 1];
    const u32 sa = abase + w * NH * 4096, sb = bbase + w * NH * 5120;
#pragma unroll
    for (int s = 0; s < DEP; ++s) ld(st[s], sa + s * 4096, sb + s * 5120);
#pragma unroll
    for (int i = 0; i < 4; ++i)
#pragma unroll
      for (int n = 0; n < 5; ++n) acc[i][n] = f4{0.f, 0.f, 0.f, 0.f};
    SB0();
#pragma unroll
    for (int s = 0; s < NH; ++s) {
      if (s + DEP < NH) ld(st[(s + DEP) % (DEP + 1)], sa + (s + DEP) * 4096, sb + (s + DEP) * 5120);
      if (s + DEP == NH) {
#pragma unroll
        for (int i = 0; i < 4; ++i) gs[i] = __builtin_bit_cast(float, __builtin_amdgcn_raw_buffer_load_b32(rs, r16 * 4 + i * 64, GS_OFF + rt * 256, 0));
      }
      SB0();
      u4 fa[4];
      unpair(st[s % (DEP + 1)].a, fa);
      step_mma(acc, fa, st[s % (DEP + 1)].b);
      SB0();
    }
#pragma unroll
    for (int i = 0; i < 4; ++i) {
      const float f = 1.f - gs[i];
#pragma unroll
      for (int n = 0; n < 5; ++n) acc[i][n] *= f;
    }
    STAMP(1, 3);
  } else {
    const int wo_ = w - 4;
    if (w == 7) {
      u4 pv[8][2][2], wt[5];
      const u32 o0 = r16 * 80 + g * 32, o1 = o0 + 1280;
      const u32 o00 = g < 3 ? o0 : OOB, o01 = g < 2 ? o0 + 16 : OOB, o10 = (g < 3 && r16 < 4) ? o1 : OOB, o11 = (g < 2 && r16 < 4) ? o1 + 16 : OOB;
#pragma unroll
      for (int r = 0; r < 8; ++r) {
        const u32 so = KVP_OFF + (r * 32 + rt) * 1600;
        pv[r][0][0] = BL128(rs, o00, so, 0); pv[r][0][1] = BL128(rs, o01, so, 0);
        pv[r][1][0] = BL128(rs, o10, so, 0); pv[r][1][1] = BL128(rs, o11, so, 0);
      }
#pragma unroll
      for (int nb = 0; nb < 5; ++nb) wt[nb] = BL128(rs, vo, WOT_OFF + (rt * 40 + ct * 5 + nb) * 1024, 0);
      SB0();
#pragma unroll
      for (int blk = 0; blk < 2; ++blk) {
        f4 s0 = __builtin_bit_cast(f4, pv[0][blk][0]), s1 = __builtin_bit_cast(f4, pv[0][blk][1]);
#pragma unroll
        for (int r = 1; r < 8; ++r) { s0 += __builtin_bit_cast(f4, pv[r][blk][0]); s1 += __builtin_bit_cast(f4, pv[r][blk][1]); }
        const u2 a0 = pk4(s0), a1 = pk4(s1);
        const u4 a = {a0[0], a0[1], a1[0], a1[1]};
        const int q = 5 * rt + 4 * blk + g;
        const u32 dst = (blk == 0 || g == 0) ? (KH + (q >> 3)) * 5120 + ((q & 3) * 16 + r16) * 16 + ((q >> 2) & 1) * 8 : OOB;
#pragma unroll
        for (int nb = 0; nb < 5; ++nb) {
          const f4 m = mfma(a, wt[nb], f4{0.f, 0.f, 0.f, 0.f});
          __builtin_amdgcn_raw_buffer_store_b64(pk4(m), rs, dst, bbase + nb * 1024, AUX_SC1);
        }
      }
      VMCNT(0);
      if (lane == 0) __hip_atomic_store((GAS u32*)(ws + FLAG_OFF + (rt * 8 + ct) * 64), 1u, __ATOMIC_RELAXED, __HIP_MEMORY_SCOPE_AGENT);
      SB0();
    }
    STAMP(1, 1);
    const u32 sa = abase + (4 * NH + wo_ * NO) * 4096, sb = bbase + (4 * NH + wo_ * NO) * 5120;
    const u32 zl = w * 20480, sl = zl + 512;
    u4 qf[5][4];
    GAS u32* flag = (GAS u32*)(ws + FLAG_OFF + ((8 * wo_ + (lane & 7)) * 8 + ct) * 64);
    u32 fl;
    {
      Stage st[3];
      u4 zp[8];
      const u32 zo = (lane < 40 ? lane : 39) * 16;
#pragma unroll
      for (int r = 0; r < 8; ++r) zp[r] = BL128(rs, zo, ZP_OFF + r * 2560 + wo_ * 640, 0);
#pragma unroll
      for (int i = 0; i < 4; ++i) gs[i] = __builtin_bit_cast(float, __builtin_amdgcn_raw_buffer_load_b32(rs, r16 * 4 + i * 64, GS_OFF + rt * 256, 0));
      SB0();
      ld(st[0], sa, sb);
      ld(st[1], w == 7 ? OOB : sa + 4096, w == 7 ? OOB : sb + 5120);
      ld(st[2], w == 7 ? OOB : sa + 2 * 4096, w == 7 ? OOB : sb + 2 * 5120);
      SB0();
      f4 z = __builtin_bit_cast(f4, zp[0]);
#pragma unroll
      for (int r = 1; r < 8; ++r) z += __builtin_bit_cast(f4, zp[r]);
      *(LAS u2*)(zl + lane * 8) = pk4(z);
#pragma unroll
      for (int i = 0; i < 4; ++i)
#pragma unroll
        for (int n = 0; n < 5; ++n) acc[i][n] = f4{0.f, 0.f, 0.f, 0.f};
      SB0();
      u4 fa[4];
      unpair(st[0].a, fa); step_mma(acc, fa, st[0].b);
      SB0();
#pragma unroll
      for (int t = 0; t < 2; ++t)
#pragma unroll
        for (int i = 0; i < 4; ++i) qf[t][i] = BL128(rs, vo + i * 1024, abase + (KH + 5 * wo_ + t) * 4096, 0);
      SB0();
      unpair(st[1].a, fa); step_mma(acc, fa, st[1].b);
      SB0();
#pragma unroll
      for (int t = 2; t < 5; ++t)
#pragma unroll
        for (int i = 0; i < 4; ++i) qf[t][i] = BL128(rs, vo + i * 1024, abase + (KH + 5 * wo_ + t) * 4096, 0);
      fl = __hip_atomic_load(flag, __ATOMIC_RELAXED, __HIP_MEMORY_SCOPE_AGENT);
      SB0();
      unpair(st[2].a, fa); step_mma(acc, fa, st[2].b);
#pragma unroll
      for (int i = 0; i < 4; ++i) {
        const float f = 1.f - gs[i];
#pragma unroll
        for (int n = 0; n < 5; ++n) acc[i][n] *= f;
      }
      SB0();
    }
    STAMP(1, 2);
    for (u32 spin = 0; !__all(fl == 1u) && spin < (1u << 22); ++spin) {
      __builtin_amdgcn_s_sleep(1);
      fl = __hip_atomic_load(flag, __ATOMIC_RELAXED, __HIP_MEMORY_SCOPE_AGENT);
    }
    __builtin_amdgcn_fence(__ATOMIC_ACQUIRE, "wavefront");
    u4 mf[3][5];
    auto ldm = [&](int t) {
#pragma unroll
      for (int n = 0; n < 5; ++n) mf[t % 3][n] = BL128(rs, vo + (n & 3) * 1024, bbase + (KH + 5 * wo_ + t) * 5120 + (n >> 2) * 4096, AUX_SC1);
    };
    ldm(0); ldm(1);
    SB0();
    {
      f4 den[4];
#pragma unroll
      for (int i = 0; i < 4; ++i) den[i] = f4{0.f, 0.f, 0.f, 0.f};
#pragma unroll
      for (int t = 0; t < 5; ++t) {
        const int q0 = 8 * t + g, q1 = q0 + 4;
        u2 lo = *(LAS u2*)(zl + q0 * 8), hi = *(LAS u2*)(zl + q1 * 8);
        if (q0 / 5 != r16) lo = u2{0u, 0u};
        if (q1 / 5 != r16) hi = u2{0u, 0u};
        const u4 zf = {lo[0], lo[1], hi[0], hi[1]};
        u4 fa[4];
        unpair(qf[t], fa);
#pragma unroll
        for (int i = 0; i < 4; ++i) { qf[t][i] = fa[i]; den[i] = mfma(zf, fa[i], den[i]); }
        SB0();
      }
#pragma unroll
      for (int i = 0; i < 4; ++i) {
        f4 sc;
#pragma unroll
        for (int e = 0; e < 4; ++e) sc[e] = gs[i] * __builtin_amdgcn_rcpf(den[i][e] + 1e-6f);
        *(LAS f4*)(sl + (i * 16 + r16) * 80 + g * 16) = sc;
      }
    }
    SB0();
    ldm(2);
    STAMP(1, 3);
#pragma unroll
    for (int t = 0; t < 5; ++t) {
      SB0();
      const int h0 = (8 * t + g) / 5, h1 = (8 * t + 4 + g) / 5;
#pragma unroll
      for (int i = 0; i < 4; ++i) {
        const float s0 = *(LAS float*)(sl + (i * 16 + r16) * 80 + h0 * 4), s1 = *(LAS float*)(sl + (i * 16 + r16) * 80 + h1 * 4);
        const u4 v = qf[t][i];
        qf[t][i] = u4{pk2(bflo(v[0]) * s0, bfhi(v[0]) * s0), pk2(bflo(v[1]) * s0, bfhi(v[1]) * s0),
                      pk2(bflo(v[2]) * s1, bfhi(v[2]) * s1), pk2(bflo(v[3]) * s1, bfhi(v[3]) * s1)};
      }
      step_mma(acc, qf[t], mf[t % 3]);
      SB0();
      if (t + 3 < 5) ldm(t + 3);
    }
  }
  STAMP(1, 4);
#pragma unroll
  for (int i = 0; i < 4; ++i)
#pragma unroll
    for (int n = 0; n < 5; ++n) *(LAS f4*)(w * 20480 + (i * 5 + n) * 1024 + lane * 16) = acc[i][n];
  LGKM0(); BARRIER();
  {
    const int i = w >> 1, n0 = (w & 1) * 3;
    u16* op = out + (rt * 64 + i * 16 + r16) * D_ + ct * 80 + n0 * 16 + 4 * g;
#pragma unroll
    for (int n = 0; n < 3; ++n) {
      if (n0 + n < 5) {
        const u32 a = (i * 5 + n0 + n) * 1024 + lane * 16;
        f4 v = *(LAS f4*)a;
#pragma unroll
        for (int s = 1; s < 8; ++s) v += *(LAS f4*)(a + s * 20480);
        *(u2*)(op + n * 16) = pk4(v);
      }
    }
  }
  STAMP(1, 5);
}

extern "C" void kernel_launch(void* const* d_in, const int* in_sizes, int n_in, void* d_out, int out_size, void* d_ws, size_t ws_size,
                              hipStream_t stream) {
  (void)in_sizes; (void)n_in; (void)out_size; (void)ws_size;
  (void)hipFuncSetAttribute((const void*)k1, hipFuncAttributeMaxDynamicSharedMemorySize, K1_LDS);
  (void)hipFuncSetAttribute((const void*)k2, hipFuncAttributeMaxDynamicSharedMemorySize, K2_LDS);
  k1<<<256, 512, K1_LDS, stream>>>((const u16*)d_in[0], (const u16*)d_in[1], (const u16*)d_in[2], (const u16*)d_in[4], (const u16*)d_in[5],
                                   (const u16*)d_in[6], (const u16*)d_in[7], (char*)d_ws);
  k2<<<256, 512, K2_LDS, stream>>>((char*)d_ws, (u16*)d_out);
}
```

```cpp
#include <hip/hip_runtime.h>
#include <stdint.h>

typedef unsigned short u16;
typedef unsigned int u32;
typedef unsigned long long u64;
typedef short s4 __attribute__((ext_vector_type(4)));
typedef short s8 __attribute__((ext_vector_type(8)));
typedef __bf16 bf8 __attribute__((ext_vector_type(8)));
typedef __bf16 bf2 __attribute__((ext_vector_type(2)));
typedef float f2 __attribute__((ext_vector_type(2)));
typedef float f4 __attribute__((ext_vector_type(4)));
typedef u32 u4 __attribute__((ext_vector_type(4)));
typedef u32 u2 __attribute__((ext_vector_type(2)));
#define LAS __attribute__((address_space(3)))
#define GAS __attribute__((address_space(1)))
#define DEV __device__ __forceinline__
#define SB0() __builtin_amdgcn_sched_barrier(0)

constexpr int S_ = 2048, D_ = 640, F_ = 1728;
constexpr int KS2 = 74, KH = 54;
constexpr u32 AF_OFF = 0, AF_BYTES = 32u * KS2 * 4096u;
constexpr u32 BF_OFF = AF_OFF + AF_BYTES, BF_BYTES = 8u * KS2 * 5120u;
constexpr u32 WOT_OFF = BF_OFF + BF_BYTES, WOT_BYTES = 32u * 40u * 1024u;
constexpr u32 KVP_OFF = WOT_OFF + WOT_BYTES, KVP_BYTES = 8u * 32u * 400u * 4u;
constexpr u32 ZP_OFF = KVP_OFF + KVP_BYTES, ZP_BYTES = 8u * 640u * 4u;
constexpr u32 GS_OFF = ZP_OFF + ZP_BYTES, GS_BYTES = 2048u * 4u;
constexpr u32 FLAG_OFF = GS_OFF + GS_BYTES, FLAG_BYTES = 256u * 64u;
constexpr u32 WS_BYTES = FLAG_OFF + FLAG_BYTES;
#define STAMP(k, i) do {} while (0)
#define STAMPC(k, i) do {} while (0)

constexpr u32 XBUF = 32768, WRING = 3 * XBUF, WSLOT = 12288, K1_LDS = WRING + 5 * WSLOT;
constexpr u32 K2_LDS = 163840;
constexpr u32 OOB = 0x40000000u;

DEV u4 make_srd(const void* p, u32 bytes) {
  u64 a = (u64)p;
  u4 r = {(u32)a, (u32)(a >> 32) & 0xffffu, bytes, 0x00020000u};
  return r;
}
DEV void dma16(u32 lds, u32 voff, u4 srd, u32 soff) {
  asm volatile("s_mov_b32 m0, %0\n\ts_nop 0\n\tbuffer_load_dwordx4 %1, %2, %3 offen lds" ::"s"(lds), "v"(voff), "s"(srd), "s"(soff) : "memory");
}
DEV s4 trread(u32 a) { return __builtin_amdgcn_ds_read_tr16_b64_v4i16((LAS s4*)a); }
DEV bf8 cat8(s4 lo, s4 hi) { return __builtin_bit_cast(bf8, __builtin_shufflevector(lo, hi, 0, 1, 2, 3, 4, 5, 6, 7)); }
DEV f4 mfma(bf8 a, bf8 b, f4 c) { return __builtin_amdgcn_mfma_f32_16x16x32_bf16(a, b, c, 0, 0, 0); }
DEV f4 mfma(u4 a, u4 b, f4 c) { return mfma(__builtin_bit_cast(bf8, a), __builtin_bit_cast(bf8, b), c); }
DEV u32 pk2(float a, float b) { f2 v = {a, b}; return __builtin_bit_cast(u32, __builtin_convertvector(v, bf2)); }
DEV u2 pk4(f4 v) { u2 r = {pk2(v[0], v[1]), pk2(v[2], v[3])}; return r; }
DEV float bflo(u32 v) { return __builtin_bit_cast(float, v << 16); }
DEV float bfhi(u32 v) { return __builtin_bit_cast(float, v & 0xffff0000u); }
DEV float sigm(float x) { return __builtin_amdgcn_rcpf(1.f + __builtin_amdgcn_exp2f(-1.44269504089f * x)); }
DEV float phi(float t) {
  return __builtin_fmaxf(t + 1.f, __builtin_amdgcn_fmed3f(__builtin_amdgcn_exp2f(1.44269504089f * t), 0.f, 1.f)) + 1e-6f;
}
#define LGKM0() __builtin_amdgcn_s_waitcnt(0xC07F)
#define VMCNT(n) asm volatile("s_waitcnt vmcnt(" #n ")" ::: "memory")
#define BARRIER() do { asm volatile("" ::: "memory"); __builtin_amdgcn_s_barrier(); asm volatile("" ::: "memory"); } while (0)

struct Loop {
  u4 xsrd, wsrd;
  u32 xnrec, wnrec, xso, wso, wpitch64;
  u32 xvo[2], wvo[3];
  u32 xdst, wdst, whalf, young;
  u32 xrd[2], wrd[3];
};
struct St { u32 xa, wa[3], rw, rx, dw, dx, sw, sx, nw, nx; };
DEV u32 wrapw(u32 v) { return v >= 5 * WSLOT ? v - 5 * WSLOT : v; }

#define RPAT 1
#define DPAT 1
#define PRIO 0
constexpr int rslot(int r) {
  return RPAT == 0 ? (r < 4 ? r : 4 + (r - 4) / 2) : RPAT == 1 ? r : RPAT == 2 ? (r * 4) / 3 : RPAT == 3 ? -1 : RPAT == 4 ? (r * 5) / 4 : r + 4;
}
template <bool TR, int H>
DEV void half_step(f4 (&acc)[4][6], const bf8 (&xc)[4], const bf8 (&wc)[6], bf8 (&xn)[4], bf8 (&wn)[6], const Loop& L, St& s, int t) {
  const u32 xa = s.xa, wa[3] = {s.wa[0], s.wa[1], s.wa[2]};
  u4 srd = H == 0 ? L.wsrd : L.xsrd;
  srd[2] = H == 0 ? s.nw : s.nx;
  const u32 dst = H == 0 ? s.dw : s.dx, so = H == 0 ? s.sw : s.sx;
  auto dma1 = [&](int d) {
    if (H == 0) { if (d < 3) dma16(dst + d * 1024, L.wvo[d], srd, so); }
    else dma16(dst + d * 1024, L.xvo[d & 1], srd, so + d * 10240);
  };
  auto dma = [&]() {
#pragma unroll
    for (int d = 0; d < 4; ++d) dma1(d);
  };
  s4 lo[6];
  auto rd = [&](int r) {
    if (r < 4) xn[r] = *(LAS bf8*)(xa + r * 2048);
    else { const int nb = (r - 4) >> 1; const u32 a = wa[nb % 3] + (nb / 3) * 6144;
      if (r & 1) wn[nb] = cat8(lo[nb], trread(a + 768)); else lo[nb] = trread(a); }
  };
  if (DPAT == 0) { if (L.young) dma(); }
  if (DPAT == 3) dma();
  SB0();
#pragma unroll
  for (int r = 0; r < 16; ++r) if (rslot(r) < 0) rd(r);
  SB0();
#pragma unroll
  for (int k = 0; k < 24; ++k) {
    const int b = k >> 2, i = k & 3;
    acc[i][b] = TR ? mfma(wc[b], xc[i], acc[i][b]) : mfma(xc[i], wc[b], acc[i][b]);
    SB0();
#pragma unroll
    for (int r = 0; r < 16; ++r) if (rslot(r) == k) rd(r);
    if (DPAT == 1 && k % 6 == 1) dma1(k / 6);
    if (DPAT == 2 && k % 3 == 1 && k < 12) dma1(k / 3);
    if (DPAT == 5 && k % 3 == 1) { if (k < 12) { if (L.young) dma1(k / 3); } else { if (!L.young) dma1(k / 3 - 4); } }
    if (k == 21) {
      asm volatile("" : "+s"(s.rw), "+s"(s.rx));
      s.rw = wrapw(s.rw + WSLOT);
      if (H == 0) s.rx = s.rx == 2 * XBUF ? 0 : s.rx + XBUF;
      s.xa = L.xrd[H] + s.rx;
#pragma unroll
      for (int b2 = 0; b2 < 3; ++b2) s.wa[b2] = L.wrd[b2] + s.rw;
      asm volatile("" : "+v"(s.xa), "+v"(s.wa[0]), "+v"(s.wa[1]), "+v"(s.wa[2]));
    } else if (k == 11) {
      if (H == 1) {
        asm volatile("" : "+s"(s.dw), "+s"(s.sw), "+s"(t));
        s.dw = WRING + wrapw(s.dw - WRING + 2 * WSLOT); s.sw += L.wpitch64; s.nw = t < 7 ? L.wnrec : 0u;
        asm volatile("" : "+s"(s.dw), "+s"(s.sw), "+s"(s.nw));
      } else {
        asm volatile("" : "+s"(s.dx), "+s"(s.sx), "+s"(t));
        s.dx = s.dx >= 2 * XBUF ? s.dx - 2 * XBUF : s.dx + XBUF; s.sx += 128; s.nx = t < 7 ? L.xnrec : 0u;
        asm volatile("" : "+s"(s.dx), "+s"(s.sx), "+s"(s.nx));
      }
    }
    SB0();
  }
  if (DPAT == 0) { if (!L.young) dma(); }
  if (DPAT == 4) dma();
}

template <bool TR>
DEV void mainloop(f4 (&acc)[4][6], const Loop& L) {
  bf8 x0[4], w0[6], x1[4], w1[6];
  {
#pragma unroll
    for (int i = 0; i < 4; ++i) x0[i] = *(LAS bf8*)(L.xrd[0] + i * 2048);
#pragma unroll
    for (int b = 0; b < 6; ++b) { const u32 a = L.wrd[b % 3] + (b / 3) * 6144; w0[b] = cat8(trread(a), trread(a + 768)); }
  }
  St s;
  s.rw = WSLOT; s.rx = 0; s.xa = L.xrd[1];
#pragma unroll
  for (int b = 0; b < 3; ++b) s.wa[b] = L.wrd[b] + WSLOT;
  s.dw = WRING + (L.whalf ? 0 : 4 * WSLOT) + L.wdst; s.sw = L.wso + 2 * L.wpitch64; s.nw = L.wnrec;
  s.dx = 2 * XBUF + L.xdst; s.sx = L.xso + 2 * 128; s.nx = L.xnrec;
#pragma unroll 1
  for (int t = 0; t < 10; ++t) {
    SB0(); LGKM0(); BARRIER(); SB0();
    half_step<TR, 0>(acc, x0, w0, x1, w1, L, s, t);
    SB0(); VMCNT(7); LGKM0(); BARRIER(); SB0();
    half_step<TR, 1>(acc, x1, w1, x0, w0, L, s, t);
  }
  __builtin_amdgcn_s_setprio(0);
  VMCNT(0); LGKM0();
}

__global__ __launch_bounds__(512, 2) void k1(const u16* __restrict__ x, const u16* __restrict__ wqkv, const u16* __restrict__ wo,
                                             const u16* __restrict__ wg, const u16* __restrict__ wu, const u16* __restrict__ wd,
                                             const u16* __restrict__ gw, char* __restrict__ ws) {
  extern __shared__ __attribute__((aligned(16))) char smem[];
  const int tid = threadIdx.x, lane = tid & 63, w = __builtin_amdgcn_readfirstlane(tid >> 6);
  const int bid = blockIdx.x, xcd = bid & 7, jj = bid >> 3;
  const int g = lane >> 4, r16 = lane & 15;
  if (tid == 0) ((u64*)(ws + (32u << 20)))[bid] = __builtin_amdgcn_s_memrealtime();
  if (jj >= 30) {
    const int tw = (jj - 30) * 8 + xcd;
    if (tw == 0 && tid < 256) *(u32*)(ws + FLAG_OFF + tid * 64) = 0u;
    u32 vo[5];
#pragma unroll
    for (int i = 0; i < 5; ++i) { const int n = i * 64 + lane, row = 4 * w + n / 80, p = n % 80; vo[i] = row * 1280 + ((p ^ ((row & 7) << 1)) << 4); }
    const u4 wdsrd = make_srd(wd, F_ * D_ * 2), wosrd = make_srd(wo, D_ * D_ * 2);
    asm volatile("s_nop 4");
#pragma unroll
    for (int s = 0; s < 3; ++s)
#pragma unroll
      for (int i = 0; i < 5; ++i) dma16(s * 40960 + w * 5120 + i * 1024, vo[i], wdsrd, (tw + 16 * s) * 40960);
    {
      const int row = tw * 128 + w * 16 + (lane >> 2);
      const u4* xp = (const u4*)(x + row * D_) + (lane & 3);
      const u4* gp = (const u4*)gw + (lane & 3);
      float a0 = 0.f, a1 = 0.f;
#pragma unroll
      for (int c = 0; c < 20; ++c) {
        const u4 xv = xp[4 * c], gv = gp[4 * c];
#pragma unroll
        for (int e = 0; e < 4; ++e) { a0 += bflo(xv[e]) * bflo(gv[e]); a1 += bfhi(xv[e]) * bfhi(gv[e]); }
      }
      a0 += a1;
      a0 += __shfl_xor(a0, 1); a0 += __shfl_xor(a0, 2);
      if ((lane & 3) == 0) ((float*)(ws + GS_OFF))[row] = sigm(a0);
    }
    const int q = r16 >> 2, p = lane & 3;
    auto tile_wd = [&](u32 slab, int t) {
#pragma unroll
      for (int nb = 0; nb < 5; ++nb) {
        const int ch = 2 * (w * 5 + nb) + (p >> 1), row = 4 * g + q;
        const u32 a = slab + row * 1280 + ((ch ^ ((row & 7) << 1)) << 4) + 8 * (p & 1);
        const bf8 v = cat8(trread(a), trread(a + 16 * 1280));
        *(bf8*)(ws + BF_OFF + ((w * KS2 + t) * 5 + nb) * 1024 + lane * 16) = v;
      }
    };
    VMCNT(0); BARRIER();
#pragma unroll
    for (int s = 0; s < 3; ++s) tile_wd(s * 40960, tw + 16 * s);
    LGKM0(); BARRIER();
    if (tw < 6) {
#pragma unroll
      for (int i = 0; i < 5; ++i) dma16(w * 5120 + i * 1024, vo[i], wdsrd, (tw + 48) * 40960);
    }
    if (w < 5) {
#pragma unroll
      for (int s = 0; s < 2; ++s)
#pragma unroll
        for (int i = 0; i < 5; ++i) dma16(40960 + s * 25600 + w * 5120 + i * 1024, vo[i], wosrd, (tw * 2 + s) * 25600);
    }
    VMCNT(0); BARRIER();
    if (tw < 6) tile_wd(0, tw + 48);
#pragma unroll
    for (int s = 0; s < 2; ++s)
#pragma unroll
      for (int nb = 0; nb < 5; ++nb) {
        const int ch = 2 * (w * 5 + nb) + (p >> 1);
        s4 v[2];
#pragma unroll
        for (int t = 0; t < 2; ++t) {
          const int e0 = 8 * g + 4 * t, row = (e0 < 20 ? e0 : 0) + q;
          v[t] = trread(40960 + s * 25600 + row * 1280 + ((ch ^ ((row & 7) << 1)) << 4) + 8 * (p & 1));
          if (e0 >= 20) v[t] = s4{0, 0, 0, 0};
        }
        *(bf8*)(ws + WOT_OFF + ((tw * 2 + s) * 40 + w * 5 + nb) * 1024 + lane * 16) = cat8(v[0], v[1]);
      }
    STAMP(0, 3);
    return;
  }
  const int idx = (xcd >> 1) * 30 + jj, rt = (xcd & 1) * 4 + (idx & 3);
  Loop L;
  L.xnrec = S_ * D_ * 2;
  L.xsrd = make_srd(x, L.xnrec);
  L.xso = (rt * 256 + w * 32) * 1280;
#pragma unroll
  for (int i = 0; i < 2; ++i) L.xvo[i] = (lane >> 3) * 1280 + (((lane & 7) ^ ((4 * i + (lane >> 4)) & 7)) << 4);
  L.xdst = w * 4096;
  asm volatile("s_nop 4");
#pragma unroll
  for (int i = 0; i < 4; ++i) dma16(L.xdst + i * 1024, L.xvo[i & 1], L.xsrd, L.xso + i * 10240);
  SB0();
  const int cq = idx >> 2, c15 = cq >= 15 ? cq - 15 : cq, up = cq >= 15;
  const int r8 = c15 & 7, h8 = c15 >> 3;
  const int type = (r8 == 2 || r8 == 5) ? 1 : (r8 == 6 ? 2 : 0);
  const int c = type == 0 ? up * 9 + h8 * 5 + (r8 - (r8 > 2) - (r8 > 5) - (r8 > 6)) : (type == 1 ? up * 4 + h8 * 2 + (r8 == 5) : up * 2 + h8);
  const int wm = w >> 1, j = w & 1;
  {
    const int part = w >> 2, wq = w & 3;
    const u32 pitch = type == 0 ? F_ * 2 : 3 * D_ * 2;
    const u32 colbase = type == 0 ? 96 * c : (type == 1 ? (part ? 1280 : 640) + 80 * c : 192 * c + 96 * part);
    const int nch = type == 0 ? 12 : (type == 1 ? 10 : (c < 3 ? 12 : (part ? 0 : 8)));
    L.wnrec = D_ * pitch;
    L.wsrd = make_srd(type == 0 ? (part ? wu : wg) : wqkv, L.wnrec);
    L.wpitch64 = 64 * pitch;
    L.wso = 16 * wq * pitch + colbase * 2;
#pragma unroll
    for (int i = 0; i < 3; ++i) {
      const int n = i * 64 + lane, row = n / 12, ch = (n % 12) ^ (((row >> 3) & 1) << 1);
      L.wvo[i] = ch < nch ? row * pitch + (ch << 4) : OOB;
    }
    L.whalf = wq >> 1;
    L.wdst = part * 6144 + (wq & 1) * 3072;
    L.young = part;
  }
  asm volatile("s_nop 4");
#pragma unroll
  for (int i = 0; i < 3; ++i) dma16(WRING + L.whalf * WSLOT + L.wdst + i * 1024, L.wvo[i], L.wsrd, L.wso);
  BARRIER();
#pragma unroll
  for (int i = 0; i < 4; ++i) dma16(XBUF + L.xdst + i * 1024, L.xvo[i & 1], L.xsrd, L.xso + i * 10240 + 128);
#pragma unroll
  for (int i = 0; i < 3; ++i) dma16(WRING + (2 + L.whalf) * WSLOT + L.wdst + i * 1024, L.wvo[i], L.wsrd, L.wso + L.wpitch64);
#pragma unroll
  for (int i = 0; i < 4; ++i) dma16(2 * XBUF + L.xdst + i * 1024, L.xvo[i & 1], L.xsrd, L.xso + i * 10240 + 256);
  SB0();
  {
#pragma unroll
    for (int s = 0; s < 2; ++s) L.xrd[s] = (64 * wm + r16) * 128 + (((4 * s + g) ^ (r16 >> 1)) << 4);
    const int q = r16 >> 2, p = lane & 3;
#pragma unroll
    for (int b = 0; b < 3; ++b) {
      const int ch = (type == 1 ? 5 : 6) * j + 2 * b;
      L.wrd[b] = WRING + (8 * g + q) * 192 + (((ch + (p >> 1)) ^ ((g & 1) << 1)) << 4) + 8 * (p & 1);
    }
  }
  f4 acc[4][6];
#pragma unroll
  for (int i = 0; i < 4; ++i)
#pragma unroll
    for (int b = 0; b < 6; ++b) acc[i][b] = f4{0.f, 0.f, 0.f, 0.f};
  VMCNT(11); BARRIER();
  STAMP(0, 1); STAMPC(0, 6);
  char* af = ws + AF_OFF + (u32)(rt * 4 + wm) * (KS2 * 4096u) + lane * 16;
  if (type != 1) {
    mainloop<true>(acc, L);
    STAMP(0, 2); STAMPC(0, 7);
    if (type == 0) {
#pragma unroll
      for (int b = 0; b < 3; ++b)
#pragma unroll
        for (int ip = 0; ip < 2; ++ip) {
          f4 h0, h1;
#pragma unroll
          for (int e = 0; e < 4; ++e) {
            h0[e] = sigm(acc[2 * ip][b][e]) * acc[2 * ip][b + 3][e];
            h1[e] = sigm(acc[2 * ip + 1][b][e]) * acc[2 * ip + 1][b + 3][e];
          }
          const u2 v0 = pk4(h0), v1 = pk4(h1);
          *(u4*)(af + (6 * c + 3 * j + b) * 2048 + ip * 1024) = u4{v0[0], v0[1], v1[0], v1[1]};
        }
    } else {
#pragma unroll
      for (int b = 0; b < 6; ++b) {
        const int blk = 12 * c + 6 * (b / 3) + 3 * j + b % 3;
        if (blk < 40) {
#pragma unroll
          for (int ip = 0; ip < 2; ++ip) {
            f4 h0, h1;
#pragma unroll
            for (int e = 0; e < 4; ++e) { h0[e] = phi(acc[2 * ip][b][e]); h1[e] = phi(acc[2 * ip + 1][b][e]); }
            const u2 v0 = pk4(h0), v1 = pk4(h1);
            *(u4*)(af + (2 * KH + blk) * 2048 + ip * 1024) = u4{v0[0], v0[1], v1[0], v1[1]};
          }
        }
      }
    }
  } else {
    mainloop<false>(acc, L);
    STAMP(0, 2); STAMPC(0, 7);
    constexpr int PA[8] = {0, 0, 1, 1, 1, 2, 2, 0}, PB[8] = {0, 1, 0, 1, 2, 1, 2, 2};
    f4 kv[8];
#pragma unroll
    for (int n = 0; n < 8; ++n) kv[n] = f4{0.f, 0.f, 0.f, 0.f};
#pragma unroll
    for (int s = 0; s < 2; ++s) {
      u4 pk[3], vv[3];
#pragma unroll
      for (int b = 0; b < 3; ++b) {
        f4 p0, p1;
#pragma unroll
        for (int e = 0; e < 4; ++e) { p0[e] = phi(acc[2 * s][b][e]); p1[e] = phi(acc[2 * s + 1][b][e]); }
        const u2 a0 = pk4(p0), a1 = pk4(p1), b0 = pk4(acc[2 * s][3 + b]), b1 = pk4(acc[2 * s + 1][3 + b]);
        pk[b] = u4{a0[0], a0[1], a1[0], a1[1]};
        vv[b] = u4{b0[0], b0[1], b1[0], b1[1]};
      }
      if (r16 == 8) vv[2] = u4{0x3f803f80u, 0x3f803f80u, 0x3f803f80u, 0x3f803f80u};
#pragma unroll
      for (int n = 0; n < 8; ++n) kv[n] = mfma(vv[PB[n]], pk[PA[n]], kv[n]);
    }
    BARRIER();
#pragma unroll
    for (int n = 0; n < 8; ++n) *(LAS f4*)((w * 8 + n) * 1024 + lane * 16) = kv[n];
    LGKM0(); BARRIER();
    const int pa = (0x02211100 >> (4 * w)) & 15, pb = (0x22121010 >> (4 * w)) & 15;
    const int dj = 16 * pa + r16, ej = 16 * pb + 4 * g, hh = dj >= 20;
#pragma unroll
    for (int j2 = 0; j2 < 2; ++j2) {
      f4 sum = *(LAS f4*)((j2 * 8 + w) * 1024 + lane * 16);
#pragma unroll
      for (int m = 1; m < 4; ++m) sum += *(LAS f4*)(((2 * m + j2) * 8 + w) * 1024 + lane * 16);
      const int head = 4 * c + 2 * j2 + hh;
      if (dj < 40) {
        if (ej < 40 && (ej >= 20) == hh) *(f4*)(ws + KVP_OFF + ((rt * 32 + head) * 400 + (dj - 20 * hh) * 20 + (ej - 20 * hh)) * 4) = sum;
        if (ej == 40) ((float*)(ws + ZP_OFF))[rt * 640 + c * 80 + 40 * j2 + dj] = sum[0];
      }
    }
  }
  STAMP(0, 3);
}

constexpr int AUX_SC1 = 16;
#define BL128(rs, vo, so, aux) __builtin_amdgcn_raw_buffer_load_b128(rs, vo, so, aux)
struct Stage { u4 a[4], b[5]; };
DEV void unpair(const u4 (&p)[4], u4 (&f)[4]) {
#pragma unroll
  for (int ip = 0; ip < 2; ++ip) {
    f[2 * ip] = u4{p[ip][0], p[ip][1], p[2 + ip][0], p[2 + ip][1]};
    f[2 * ip + 1] = u4{p[ip][2], p[ip][3], p[2 + ip][2], p[2 + ip][3]};
  }
}
DEV void step_mma(f4 (&acc)[4][5], const u4 (&a)[4], const u4 (&b)[5]) {
#pragma unroll
  for (int n = 0; n < 5; ++n)
#pragma unroll
    for (int i = 0; i < 4; ++i) acc[i][n] = mfma(b[n], a[i], acc[i][n]);
}
constexpr int NH = 11, NO = 3;

__global__ __launch_bounds__(512, 2) void k2(char* __restrict__ ws, u16* __restrict__ out) {
  extern __shared__ __attribute__((aligned(16))) char smem[];
  const int tid = threadIdx.x, lane = tid & 63, w = __builtin_amdgcn_readfirstlane(tid >> 6);
  const int bid = blockIdx.x, xcd = bid & 7, jj = bid >> 3;
  const int rt = (xcd >> 1) * 8 + (jj >> 2), ct = (xcd & 1) * 4 + (jj & 3);
  const int g = lane >> 4, r16 = lane & 15;
  const auto rs = __builtin_amdgcn_make_buffer_rsrc(ws, 0, WS_BYTES, 0x00020000);
  const u32 vo = lane * 16;
  const u32 abase = AF_OFF + rt * (KS2 * 4096u), bbase = BF_OFF + ct * (KS2 * 5120u);
  STAMP(1, 0);
  auto ld = [&](Stage& st, u32 sa, u32 sb) {
#pragma unroll
    for (int i = 0; i < 4; ++i) st.a[i] = BL128(rs, vo + i * 1024, sa, 0);
#pragma unroll
    for (int n = 0; n < 5; ++n) st.b[n] = BL128(rs, vo + (n & 3) * 1024, sb + (n >> 2) * 4096, 0);
  };
  f4 acc[4][5];
  float gs[4];
  if (w < 4) {
    constexpr int DEP = 3;
    Stage st[DEP + 1];
    const u32 sa = abase + w * NH * 4096, sb = bbase + w * NH * 5120;
#pragma unroll
    for (int s = 0; s < DEP; ++s) ld(st[s], sa + s * 4096, sb + s * 5120);
#pragma unroll
    for (int i = 0; i < 4; ++i)
#pragma unroll
      for (int n = 0; n < 5; ++n) acc[i][n] = f4{0.f, 0.f, 0.f, 0.f};
    SB0();
#pragma unroll
    for (int s = 0; s < NH; ++s) {
      if (s + DEP < NH) ld(st[(s + DEP) % (DEP + 1)], sa + (s + DEP) * 4096, sb + (s + DEP) * 5120);
      if (s + DEP == NH) {
#pragma unroll
        for (int i = 0; i < 4; ++i) gs[i] = __builtin_bit_cast(float, __builtin_amdgcn_raw_buffer_load_b32(rs, r16 * 4 + i * 64, GS_OFF + rt * 256, 0));
      }
      SB0();
      u4 fa[4];
      unpair(st[s % (DEP + 1)].a, fa);
      step_mma(acc, fa, st[s % (DEP + 1)].b);
      SB0();
    }
#pragma unroll
    for (int i = 0; i < 4; ++i) {
      const float f = 1.f - gs[i];
#pragma unroll
      for (int n = 0; n < 5; ++n) acc[i][n] *= f;
    }
    STAMP(1, 3);
  } else {
    const int wo_ = w - 4;
    if (w == 7) {
      u4 pv[8][2][2], wt[5];
      const u32 o0 = r16 * 80 + g * 32, o1 = o0 + 1280;
      const u32 o00 = g < 3 ? o0 : OOB, o01 = g < 2 ? o0 + 16 : OOB, o10 = (g < 3 && r16 < 4) ? o1 : OOB, o11 = (g < 2 && r16 < 4) ? o1 + 16 : OOB;
#pragma unroll
      for (int r = 0; r < 8; ++r) {
        const u32 so = KVP_OFF + (r * 32 + rt) * 1600;
        pv[r][0][0] = BL128(rs, o00, so, 0); pv[r][0][1] = BL128(rs, o01, so, 0);
        pv[r][1][0] = BL128(rs, o10, so, 0); pv[r][1][1] = BL128(rs, o11, so, 0);
      }
#pragma unroll
      for (int nb = 0; nb < 5; ++nb) wt[nb] = BL128(rs, vo, WOT_OFF + (rt * 40 + ct * 5 + nb) * 1024, 0);
      SB0();
#pragma unroll
      for (int blk = 0; blk < 2; ++blk) {
        f4 s0 = __builtin_bit_cast(f4, pv[0][blk][0]), s1 = __builtin_bit_cast(f4, pv[0][blk][1]);
#pragma unroll
        for (int r = 1; r < 8; ++r) { s0 += __builtin_bit_cast(f4, pv[r][blk][0]); s1 += __builtin_bit_cast(f4, pv[r][blk][1]); }
        const u2 a0 = pk4(s0), a1 = pk4(s1);
        const u4 a = {a0[0], a0[1], a1[0], a1[1]};
        const int q = 5 * rt + 4 * blk + g;
        const u32 dst = (blk == 0 || g == 0) ? (KH + (q >> 3)) * 5120 + ((q & 3) * 16 + r16) * 16 + ((q >> 2) & 1) * 8 : OOB;
#pragma unroll
        for (int nb = 0; nb < 5; ++nb) {
          const f4 m = mfma(a, wt[nb], f4{0.f, 0.f, 0.f, 0.f});
          __builtin_amdgcn_raw_buffer_store_b64(pk4(m), rs, dst, bbase + nb * 1024, AUX_SC1);
        }
      }
      VMCNT(0);
      if (lane == 0) __hip_atomic_store((GAS u32*)(ws + FLAG_OFF + (rt * 8 + ct) * 64), 1u, __ATOMIC_RELAXED, __HIP_MEMORY_SCOPE_AGENT);
      SB0();
    }
    STAMP(1, 1);
    const u32 sa = abase + (4 * NH + wo_ * NO) * 4096, sb = bbase + (4 * NH + wo_ * NO) * 5120;
    const u32 zl = w * 20480, sl = zl + 512;
    u4 qf[5][4];
    GAS u32* flag = (GAS u32*)(ws + FLAG_OFF + ((8 * wo_ + (lane & 7)) * 8 + ct) * 64);
    u32 fl;
    {
      Stage st[3];
      u4 zp[8];
      const u32 zo = (lane < 40 ? lane : 39) * 16;
#pragma unroll
      for (int r = 0; r < 8; ++r) zp[r] = BL128(rs, zo, ZP_OFF + r * 2560 + wo_ * 640, 0);
#pragma unroll
      for (int i = 0; i < 4; ++i) gs[i] = __builtin_bit_cast(float, __builtin_amdgcn_raw_buffer_load_b32(rs, r16 * 4 + i * 64, GS_OFF + rt * 256, 0));
      SB0();
      ld(st[0], sa, sb);
      ld(st[1], w == 7 ? OOB : sa + 4096, w == 7 ? OOB : sb + 5120);
      ld(st[2], w == 7 ? OOB : sa + 2 * 4096, w == 7 ? OOB : sb + 2 * 5120);
      SB0();
      f4 z = __builtin_bit_cast(f4, zp[0]);
#pragma unroll
      for (int r = 1; r < 8; ++r) z += __builtin_bit_cast(f4, zp[r]);
      *(LAS u2*)(zl + lane * 8) = pk4(z);
#pragma unroll
      for (int i = 0; i < 4; ++i)
#pragma unroll
        for (int n = 0; n < 5; ++n) acc[i][n] = f4{0.f, 0.f, 0.f, 0.f};
      SB0();
      u4 fa[4];
      unpair(st[0].a, fa); step_mma(acc, fa, st[0].b);
      SB0();
#pragma unroll
      for (int t = 0; t < 2; ++t)
#pragma unroll
        for (int i = 0; i < 4; ++i) qf[t][i] = BL128(rs, vo + i * 1024, abase + (KH + 5 * wo_ + t) * 4096, 0);
      SB0();
      unpair(st[1].a, fa); step_mma(acc, fa, st[1].b);
      SB0();
#pragma unroll
      for (int t = 2; t < 5; ++t)
#pragma unroll
        for (int i = 0; i < 4; ++i) qf[t][i] = BL128(rs, vo + i * 1024, abase + (KH + 5 * wo_ + t) * 4096, 0);
      fl = __hip_atomic_load(flag, __ATOMIC_RELAXED, __HIP_MEMORY_SCOPE_AGENT);
      SB0();
      unpair(st[2].a, fa); step_mma(acc, fa, st[2].b);
#pragma unroll
      for (int i = 0; i < 4; ++i) {
        const float f = 1.f - gs[i];
#pragma unroll
        for (int n = 0; n < 5; ++n) acc[i][n] *= f;
      }
      SB0();
    }
    STAMP(1, 2);
    for (u32 spin = 0; !__all(fl == 1u) && spin < (1u << 22); ++spin) {
      __builtin_amdgcn_s_sleep(1);
      fl = __hip_atomic_load(flag, __ATOMIC_RELAXED, __HIP_MEMORY_SCOPE_AGENT);
    }
    __builtin_amdgcn_fence(__ATOMIC_ACQUIRE, "wavefront");
    u4 mf[3][5];
    auto ldm = [&](int t) {
#pragma unroll
      for (int n = 0; n < 5; ++n) mf[t % 3][n] = BL128(rs, vo + (n & 3) * 1024, bbase + (KH + 5 * wo_ + t) * 5120 + (n >> 2) * 4096, AUX_SC1);
    };
    ldm(0); ldm(1);
    SB0();
    {
      f4 den[4];
#pragma unroll
      for (int i = 0; i < 4; ++i) den[i] = f4{0.f, 0.f, 0.f, 0.f};
#pragma unroll
      for (int t = 0; t < 5; ++t) {
        const int q0 = 8 * t + g, q1 = q0 + 4;
        u2 lo = *(LAS u2*)(zl + q0 * 8), hi = *(LAS u2*)(zl + q1 * 8);
        if (q0 / 5 != r16) lo = u2{0u, 0u};
        if (q1 / 5 != r16) hi = u2{0u, 0u};
        const u4 zf = {lo[0], lo[1], hi[0], hi[1]};
        u4 fa[4];
        unpair(qf[t], fa);
#pragma unroll
        for (int i = 0; i < 4; ++i) { qf[t][i] = fa[i]; den[i] = mfma(zf, fa[i], den[i]); }
        SB0();
      }
#pragma unroll
      for (int i = 0; i < 4; ++i) {
        f4 sc;
#pragma unroll
        for (int e = 0; e < 4; ++e) sc[e] = gs[i] * __builtin_amdgcn_rcpf(den[i][e] + 1e-6f);
        *(LAS f4*)(sl + (i * 16 + r16) * 80 + g * 16) = sc;
      }
    }
    SB0();
    ldm(2);
    STAMP(1, 3);
#pragma unroll
    for (int t = 0; t < 5; ++t) {
      SB0();
      const int h0 = (8 * t + g) / 5, h1 = (8 * t + 4 + g) / 5;
#pragma unroll
      for (int i = 0; i < 4; ++i) {
        const float s0 = *(LAS float*)(sl + (i * 16 + r16) * 80 + h0 * 4), s1 = *(LAS float*)(sl + (i * 16 + r16) * 80 + h1 * 4);
        const u4 v = qf[t][i];
        qf[t][i] = u4{pk2(bflo(v[0]) * s0, bfhi(v[0]) * s0), pk2(bflo(v[1]) * s0, bfhi(v[1]) * s0),
                      pk2(bflo(v[2]) * s1, bfhi(v[2]) * s1), pk2(bflo(v[3]) * s1, bfhi(v[3]) * s1)};
      }
      step_mma(acc, qf[t], mf[t % 3]);
      SB0();
      if (t + 3 < 5) ldm(t + 3);
    }
  }
  STAMP(1, 4);
#pragma unroll
  for (int i = 0; i < 4; ++i)
#pragma unroll
    for (int n = 0; n < 5; ++n) *(LAS f4*)(w * 20480 + (i * 5 + n) * 1024 + lane * 16) = acc[i][n];
  LGKM0(); BARRIER();
  {
    const int i = w >> 1, n0 = (w & 1) * 3;
    u16* op = out + (rt * 64 + i * 16 + r16) * D_ + ct * 80 + n0 * 16 + 4 * g;
#pragma unroll
    for (int n = 0; n < 3; ++n) {
      if (n0 + n < 5) {
        const u32 a = (i * 5 + n0 + n) * 1024 + lane * 16;
        f4 v = *(LAS f4*)a;
#pragma unroll
        for (int s = 1; s < 8; ++s) v += *(LAS f4*)(a + s * 20480);
        *(u2*)(op + n * 16) = pk4(v);
      }
    }
  }
  STAMP(1, 5);
}

__global__ void klast(char* ws) {
  const int lane = threadIdx.x;
    u64 m = ~0ull;
    for (int i = 0; i < 4; ++i) { u64 v = ((u64*)(ws + (32u << 20)))[lane * 4 + i]; m = v < m ? v : m; }
    for (int o = 1; o < 64; o <<= 1) { u64 v = __shfl_xor(m, o); m = v < m ? v : m; }
    while (__builtin_amdgcn_s_memrealtime() - m < 6000ull) __builtin_amdgcn_s_sleep(1);
}
extern "C" void kernel_launch(void* const* d_in, const int* in_sizes, int n_in, void* d_out, int out_size, void* d_ws, size_t ws_size,
                              hipStream_t stream) {
  (void)in_sizes; (void)n_in; (void)out_size; (void)ws_size;
  (void)hipFuncSetAttribute((const void*)k1, hipFuncAttributeMaxDynamicSharedMemorySize, K1_LDS);
  (void)hipFuncSetAttribute((const void*)k2, hipFuncAttributeMaxDynamicSharedMemorySize, K2_LDS);
  k1<<<256, 512, K1_LDS, stream>>>((const u16*)d_in[0], (const u16*)d_in[1], (const u16*)d_in[2], (const u16*)d_in[4], (const u16*)d_in[5],
                                   (const u16*)d_in[6], (const u16*)d_in[7], (char*)d_ws);
  k2<<<256, 512, K2_LDS, stream>>>((char*)d_ws, (u16*)d_out);
  klast<<<1, 64, 0, stream>>>((char*)d_ws);
}
```

```cpp
#include <hip/hip_runtime.h>
#include <stdint.h>

typedef unsigned short u16;
typedef unsigned int u32;
typedef unsigned long long u64;
typedef short s4 __attribute__((ext_vector_type(4)));
typedef short s8 __attribute__((ext_vector_type(8)));
typedef __bf16 bf8 __attribute__((ext_vector_type(8)));
typedef __bf16 bf2 __attribute__((ext_vector_type(2)));
typedef float f2 __attribute__((ext_vector_type(2)));
typedef float f4 __attribute__((ext_vector_type(4)));
typedef u32 u4 __attribute__((ext_vector_type(4)));
typedef u32 u2 __attribute__((ext_vector_type(2)));
#define LAS __attribute__((address_space(3)))
#define GAS __attribute__((address_space(1)))
#define DEV __device__ __forceinline__
#define SB0() __builtin_amdgcn_sched_barrier(0)

constexpr int S_ = 2048, D_ = 640, F_ = 1728;
constexpr int KS2 = 74, KH = 54;
constexpr u32 AF_OFF = 0, AF_BYTES = 32u * KS2 * 4096u;
constexpr u32 BF_OFF = AF_OFF + AF_BYTES, BF_BYTES = 8u * KS2 * 5120u;
constexpr u32 WOT_OFF = BF_OFF + BF_BYTES, WOT_BYTES = 32u * 40u * 1024u;
constexpr u32 KVP_OFF = WOT_OFF + WOT_BYTES, KVP_BYTES = 8u * 32u * 400u * 4u;
constexpr u32 ZP_OFF = KVP_OFF + KVP_BYTES, ZP_BYTES = 8u * 640u * 4u;
constexpr u32 GS_OFF = ZP_OFF + ZP_BYTES, GS_BYTES = 2048u * 4u;
constexpr u32 FLAG_OFF = GS_OFF + GS_BYTES, FLAG_BYTES = 513u * 64u;
constexpr int FL_G = 0, FL_KV = 144, FL_Q = 208, FL_T = 240, FL_M = 256, FL_EPOCH = 512;
constexpr u32 WS_BYTES = FLAG_OFF + FLAG_BYTES;
#define STAMP(k, i) do {} while (0)
#define STAMPC(k, i) do {} while (0)

constexpr u32 XBUF = 32768, WRING = 3 * XBUF, WSLOT = 12288, K1_LDS = WRING + 5 * WSLOT;
constexpr u32 K2_LDS = 163840;
constexpr u32 OOB = 0x40000000u;
constexpr int AUX_SC1 = 16;
#define BL128(rs, vo, so, aux) __builtin_amdgcn_raw_buffer_load_b128(rs, vo, so, aux)
#define STAUX AUX_SC1
#define BS128(v, rs, vo, so) __builtin_amdgcn_raw_buffer_store_b128(__builtin_bit_cast(u4, v), rs, (vo) + (so), 0, STAUX)
#define LDAUX AUX_SC1
#define RLX_AGENT __ATOMIC_RELAXED, __HIP_MEMORY_SCOPE_AGENT

DEV u4 make_srd(const void* p, u32 bytes) {
  u64 a = (u64)p;
  u4 r = {(u32)a, (u32)(a >> 32) & 0xffffu, bytes, 0x00020000u};
  return r;
}
DEV void dma16(u32 lds, u32 voff, u4 srd, u32 soff) {
  asm volatile("s_mov_b32 m0, %0\n\ts_nop 0\n\tbuffer_load_dwordx4 %1, %2, %3 offen lds" ::"s"(lds), "v"(voff), "s"(srd), "s"(soff) : "memory");
}
DEV s4 trread(u32 a) { return __builtin_amdgcn_ds_read_tr16_b64_v4i16((LAS s4*)a); }
DEV bf8 cat8(s4 lo, s4 hi) { return __builtin_bit_cast(bf8, __builtin_shufflevector(lo, hi, 0, 1, 2, 3, 4, 5, 6, 7)); }
DEV f4 mfma(bf8 a, bf8 b, f4 c) { return __builtin_amdgcn_mfma_f32_16x16x32_bf16(a, b, c, 0, 0, 0); }
DEV f4 mfma(u4 a, u4 b, f4 c) { return mfma(__builtin_bit_cast(bf8, a), __builtin_bit_cast(bf8, b), c); }
DEV u32 pk2(float a, float b) { f2 v = {a, b}; return __builtin_bit_cast(u32, __builtin_convertvector(v, bf2)); }
DEV u2 pk4(f4 v) { u2 r = {pk2(v[0], v[1]), pk2(v[2], v[3])}; return r; }
DEV float bflo(u32 v) { return __builtin_bit_cast(float, v << 16); }
DEV float bfhi(u32 v) { return __builtin_bit_cast(float, v & 0xffff0000u); }
DEV float sigm(float x) { return __builtin_amdgcn_rcpf(1.f + __builtin_amdgcn_exp2f(-1.44269504089f * x)); }
DEV float phi(float t) {
  return __builtin_fmaxf(t + 1.f, __builtin_amdgcn_fmed3f(__builtin_amdgcn_exp2f(1.44269504089f * t), 0.f, 1.f)) + 1e-6f;
}
#define LGKM0() __builtin_amdgcn_s_waitcnt(0xC07F)
#define VMCNT(n) asm volatile("s_waitcnt vmcnt(" #n ")" ::: "memory")
#define BARRIER() do { asm volatile("" ::: "memory"); __builtin_amdgcn_s_barrier(); asm volatile("" ::: "memory"); } while (0)

struct Loop {
  u4 xsrd, wsrd;
  u32 xnrec, wnrec, xso, wso, wpitch64;
  u32 xvo[2], wvo[3];
  u32 xdst, wdst, whalf, young;
  u32 xrd[2], wrd[3];
};
struct St { u32 xa, wa[3], rw, rx, dw, dx, sw, sx, nw, nx; };
DEV u32 wrapw(u32 v) { return v >= 5 * WSLOT ? v - 5 * WSLOT : v; }

#define RPAT 1
#define DPAT 1
#define PRIO 0
constexpr int rslot(int r) {
  return RPAT == 0 ? (r < 4 ? r : 4 + (r - 4) / 2) : RPAT == 1 ? r : RPAT == 2 ? (r * 4) / 3 : RPAT == 3 ? -1 : RPAT == 4 ? (r * 5) / 4 : r + 4;
}
template <bool TR, int H>
DEV void half_step(f4 (&acc)[4][6], const bf8 (&xc)[4], const bf8 (&wc)[6], bf8 (&xn)[4], bf8 (&wn)[6], const Loop& L, St& s, int t) {
  const u32 xa = s.xa, wa[3] = {s.wa[0], s.wa[1], s.wa[2]};
  u4 srd = H == 0 ? L.wsrd : L.xsrd;
  srd[2] = H == 0 ? s.nw : s.nx;
  const u32 dst = H == 0 ? s.dw : s.dx, so = H == 0 ? s.sw : s.sx;
  auto dma1 = [&](int d) {
    if (H == 0) { if (d < 3) dma16(dst + d * 1024, L.wvo[d], srd, so); }
    else dma16(dst + d * 1024, L.xvo[d & 1], srd, so + d * 10240);
  };
  auto dma = [&]() {
#pragma unroll
    for (int d = 0; d < 4; ++d) dma1(d);
  };
  s4 lo[6];
  auto rd = [&](int r) {
    if (r < 4) xn[r] = *(LAS bf8*)(xa + r * 2048);
    else { const int nb = (r - 4) >> 1; const u32 a = wa[nb % 3] + (nb / 3) * 6144;
      if (r & 1) wn[nb] = cat8(lo[nb], trread(a + 768)); else lo[nb] = trread(a); }
  };
  if (DPAT == 0) { if (L.young) dma(); }
  if (DPAT == 3) dma();
  SB0();
#pragma unroll
  for (int r = 0; r < 16; ++r) if (rslot(r) < 0) rd(r);
  SB0();
#pragma unroll
  for (int k = 0; k < 24; ++k) {
    const int b = k >> 2, i = k & 3;
    acc[i][b] = TR ? mfma(wc[b], xc[i], acc[i][b]) : mfma(xc[i], wc[b], acc[i][b]);
    SB0();
#pragma unroll
    for (int r = 0; r < 16; ++r) if (rslot(r) == k) rd(r);
    if (DPAT == 1 && k % 6 == 1) dma1(k / 6);
    if (DPAT == 2 && k % 3 == 1 && k < 12) dma1(k / 3);
    if (DPAT == 5 && k % 3 == 1) { if (k < 12) { if (L.young) dma1(k / 3); } else { if (!L.young) dma1(k / 3 - 4); } }
    if (k == 21) {
      asm volatile("" : "+s"(s.rw), "+s"(s.rx));
      s.rw = wrapw(s.rw + WSLOT);
      if (H == 0) s.rx = s.rx == 2 * XBUF ? 0 : s.rx + XBUF;
      s.xa = L.xrd[H] + s.rx;
#pragma unroll
      for (int b2 = 0; b2 < 3; ++b2) s.wa[b2] = L.wrd[b2] + s.rw;
      asm volatile("" : "+v"(s.xa), "+v"(s.wa[0]), "+v"(s.wa[1]), "+v"(s.wa[2]));
    } else if (k == 11) {
      if (H == 1) {
        asm volatile("" : "+s"(s.dw), "+s"(s.sw), "+s"(t));
        s.dw = WRING + wrapw(s.dw - WRING + 2 * WSLOT); s.sw += L.wpitch64; s.nw = t < 7 ? L.wnrec : 0u;
        asm volatile("" : "+s"(s.dw), "+s"(s.sw), "+s"(s.nw));
      } else {
        asm volatile("" : "+s"(s.dx), "+s"(s.sx), "+s"(t));
        s.dx = s.dx >= 2 * XBUF ? s.dx - 2 * XBUF : s.dx + XBUF; s.sx += 128; s.nx = t < 7 ? L.xnrec : 0u;
        asm volatile("" : "+s"(s.dx), "+s"(s.sx), "+s"(s.nx));
      }
    }
    SB0();
  }
  if (DPAT == 0) { if (!L.young) dma(); }
  if (DPAT == 4) dma();
}

template <bool TR>
DEV void mainloop(f4 (&acc)[4][6], const Loop& L) {
  bf8 x0[4], w0[6], x1[4], w1[6];
  {
#pragma unroll
    for (int i = 0; i < 4; ++i) x0[i] = *(LAS bf8*)(L.xrd[0] + i * 2048);
#pragma unroll
    for (int b = 0; b < 6; ++b) { const u32 a = L.wrd[b % 3] + (b / 3) * 6144; w0[b] = cat8(trread(a), trread(a + 768)); }
  }
  St s;
  s.rw = WSLOT; s.rx = 0; s.xa = L.xrd[1];
#pragma unroll
  for (int b = 0; b < 3; ++b) s.wa[b] = L.wrd[b] + WSLOT;
  s.dw = WRING + (L.whalf ? 0 : 4 * WSLOT) + L.wdst; s.sw = L.wso + 2 * L.wpitch64; s.nw = L.wnrec;
  s.dx = 2 * XBUF + L.xdst; s.sx = L.xso + 2 * 128; s.nx = L.xnrec;
#pragma unroll 1
  for (int t = 0; t < 10; ++t) {
    SB0(); LGKM0(); BARRIER(); SB0();
    half_step<TR, 0>(acc, x0, w0, x1, w1, L, s, t);
    SB0(); VMCNT(7); LGKM0(); BARRIER(); SB0();
    half_step<TR, 1>(acc, x1, w1, x0, w0, L, s, t);
  }
  __builtin_amdgcn_s_setprio(0);
  VMCNT(0); LGKM0();
}

struct Stage { u4 a[4], b[5]; };
DEV void unpair(const u4 (&p)[4], u4 (&f)[4]) {
#pragma unroll
  for (int ip = 0; ip < 2; ++ip) {
    f[2 * ip] = u4{p[ip][0], p[ip][1], p[2 + ip][0], p[2 + ip][1]};
    f[2 * ip + 1] = u4{p[ip][2], p[ip][3], p[2 + ip][2], p[2 + ip][3]};
  }
}
DEV void step_mma(f4 (&acc)[4][5], const u4 (&a)[4], const u4 (&b)[5]) {
#pragma unroll
  for (int n = 0; n < 5; ++n)
#pragma unroll
    for (int i = 0; i < 4; ++i) acc[i][n] = mfma(b[n], a[i], acc[i][n]);
}
DEV u32 flag_load(char* ws, int idx) { return __hip_atomic_load((GAS u32*)(ws + FLAG_OFF + idx * 64), RLX_AGENT); }
DEV void flag_store(char* ws, int idx, u32 v) { __hip_atomic_store((GAS u32*)(ws + FLAG_OFF + idx * 64), v, RLX_AGENT); }
DEV void wait_flags(char* ws, int idx, u32 tag, u32 v) {
  for (u32 spin = 0; !__all(v == tag) && spin < (1u << 20); ++spin) {
    __builtin_amdgcn_s_sleep(1);
    v = flag_load(ws, idx);
  }
  __builtin_amdgcn_fence(__ATOMIC_ACQUIRE, "wavefront");
}
DEV u32 load_epoch(char* ws) {
  u32 e;
  asm volatile("s_load_dword %0, %1, 0x0\n\ts_waitcnt lgkmcnt(0)" : "=s"(e) : "s"(ws + FLAG_OFF + FL_EPOCH * 64) : "memory");
  return e;
}
constexpr int NH = 11, NO = 3;

__global__ __launch_bounds__(512, 2) void fused(const u16* __restrict__ x, const u16* __restrict__ wqkv, const u16* __restrict__ wo,
                                                const u16* __restrict__ wg, const u16* __restrict__ wu, const u16* __restrict__ wd,
                                                const u16* __restrict__ gw, char* ws, u16* __restrict__ out) {
  extern __shared__ __attribute__((aligned(16))) char smem[];
  const int tid = threadIdx.x, lane = tid & 63, w = __builtin_amdgcn_readfirstlane(tid >> 6);
  const int bid = blockIdx.x, xcd = bid & 7, jj = bid >> 3;
  const int g = lane >> 4, r16 = lane & 15;
  const auto rs = __builtin_amdgcn_make_buffer_rsrc(ws, 0, WS_BYTES, 0x00020000);
  const u32 vo = lane * 16;
  u32 tag;
  int myflag;
  STAMP(0, 0);
  if (jj >= 30) {
    const int tw = (jj - 30) * 8 + xcd;
    myflag = FL_T + tw;
    u32 so[5];
#pragma unroll
    for (int i = 0; i < 5; ++i) { const int n = i * 64 + lane, row = 4 * w + n / 80, p = n % 80; so[i] = row * 1280 + ((p ^ ((row & 7) << 1)) << 4); }
    const u4 wdsrd = make_srd(wd, F_ * D_ * 2), wosrd = make_srd(wo, D_ * D_ * 2);
    asm volatile("s_nop 4");
#pragma unroll
    for (int s = 0; s < 3; ++s)
#pragma unroll
      for (int i = 0; i < 5; ++i) dma16(s * 40960 + w * 5120 + i * 1024, so[i], wdsrd, (tw + 16 * s) * 40960);
    tag = load_epoch(ws) + 1u;
    {
      const int row = tw * 128 + w * 16 + (lane >> 2);
      const u4* xp = (const u4*)(x + row * D_) + (lane & 3);
      const u4* gp = (const u4*)gw + (lane & 3);
      float a0 = 0.f, a1 = 0.f;
#pragma unroll
      for (int c = 0; c < 20; ++c) {
        const u4 xv = xp[4 * c], gv = gp[4 * c];
#pragma unroll
        for (int e = 0; e < 4; ++e) { a0 += bflo(xv[e]) * bflo(gv[e]); a1 += bfhi(xv[e]) * bfhi(gv[e]); }
      }
      a0 += a1;
      a0 += __shfl_xor(a0, 1); a0 += __shfl_xor(a0, 2);
      __builtin_amdgcn_raw_buffer_store_b32(__builtin_bit_cast(u32, sigm(a0)), rs, (lane & 3) == 0 ? row * 4 : OOB, GS_OFF, AUX_SC1);
    }
    const int q = r16 >> 2, p = lane & 3;
    auto tile_wd = [&](u32 slab, int t) {
#pragma unroll
      for (int nb = 0; nb < 5; ++nb) {
        const int ch = 2 * (w * 5 + nb) + (p >> 1), row = 4 * g + q;
        const u32 a = slab + row * 1280 + ((ch ^ ((row & 7) << 1)) << 4) + 8 * (p & 1);
        BS128(cat8(trread(a), trread(a + 16 * 1280)), rs, vo, BF_OFF + ((w * KS2 + t) * 5 + nb) * 1024);
      }
    };
    VMCNT(0); BARRIER();
#pragma unroll
    for (int s = 0; s < 3; ++s) tile_wd(s * 40960, tw + 16 * s);
    LGKM0(); BARRIER();
    if (tw < 6) {
#pragma unroll
      for (int i = 0; i < 5; ++i) dma16(w * 5120 + i * 1024, so[i], wdsrd, (tw + 48) * 40960);
    }
    if (w < 5) {
#pragma unroll
      for (int s = 0; s < 2; ++s)
#pragma unroll
        for (int i = 0; i < 5; ++i) dma16(40960 + s * 25600 + w * 5120 + i * 1024, so[i], wosrd, (tw * 2 + s) * 25600);
    }
    VMCNT(0); BARRIER();
    if (tw < 6) tile_wd(0, tw + 48);
#pragma unroll
    for (int s = 0; s < 2; ++s)
#pragma unroll
      for (int nb = 0; nb < 5; ++nb) {
        const int ch = 2 * (w * 5 + nb) + (p >> 1);
        s4 v[2];
#pragma unroll
        for (int t = 0; t < 2; ++t) {
          const int e0 = 8 * g + 4 * t, row = (e0 < 20 ? e0 : 0) + q;
          v[t] = trread(40960 + s * 25600 + row * 1280 + ((ch ^ ((row & 7) << 1)) << 4) + 8 * (p & 1));
          if (e0 >= 20) v[t] = s4{0, 0, 0, 0};
        }
        BS128(cat8(v[0], v[1]), rs, vo, WOT_OFF + ((tw * 2 + s) * 40 + w * 5 + nb) * 1024);
      }
  } else {
    const int idx = (xcd >> 1) * 30 + jj, rt = (xcd & 1) * 4 + (idx & 3);
    Loop L;
    L.xnrec = S_ * D_ * 2;
    L.xsrd = make_srd(x, L.xnrec);
    L.xso = (rt * 256 + w * 32) * 1280;
#pragma unroll
    for (int i = 0; i < 2; ++i) L.xvo[i] = (lane >> 3) * 1280 + (((lane & 7) ^ ((4 * i + (lane >> 4)) & 7)) << 4);
    L.xdst = w * 4096;
    asm volatile("s_nop 4");
#pragma unroll
    for (int i = 0; i < 4; ++i) dma16(L.xdst + i * 1024, L.xvo[i & 1], L.xsrd, L.xso + i * 10240);
    SB0();
    const int cq = idx >> 2, c15 = cq >= 15 ? cq - 15 : cq, up = cq >= 15;
    const int r8 = c15 & 7, h8 = c15 >> 3;
    const int type = (r8 == 2 || r8 == 5) ? 1 : (r8 == 6 ? 2 : 0);
    const int c = type == 0 ? up * 9 + h8 * 5 + (r8 - (r8 > 2) - (r8 > 5) - (r8 > 6)) : (type == 1 ? up * 4 + h8 * 2 + (r8 == 5) : up * 2 + h8);
    myflag = type == 0 ? FL_G + rt * 18 + c : (type == 1 ? FL_KV + rt * 8 + c : FL_Q + rt * 4 + c);
    const int wm = w >> 1, j = w & 1;
    {
      const int part = w >> 2, wq = w & 3;
      const u32 pitch = type == 0 ? F_ * 2 : 3 * D_ * 2;
      const u32 colbase = type == 0 ? 96 * c : (type == 1 ? (part ? 1280 : 640) + 80 * c : 192 * c + 96 * part);
      const int nch = type == 0 ? 12 : (type == 1 ? 10 : (c < 3 ? 12 : (part ? 0 : 8)));
      L.wnrec = D_ * pitch;
      L.wsrd = make_srd(type == 0 ? (part ? wu : wg) : wqkv, L.wnrec);
      L.wpitch64 = 64 * pitch;
      L.wso = 16 * wq * pitch + colbase * 2;
#pragma unroll
      for (int i = 0; i < 3; ++i) {
        const int n = i * 64 + lane, row = n / 12, ch = (n % 12) ^ (((row >> 3) & 1) << 1);
        L.wvo[i] = ch < nch ? row * pitch + (ch << 4) : OOB;
      }
      L.whalf = wq >> 1;
      L.wdst = part * 6144 + (wq & 1) * 3072;
      L.young = part;
    }
    asm volatile("s_nop 4");
#pragma unroll
    for (int i = 0; i < 3; ++i) dma16(WRING + L.whalf * WSLOT + L.wdst + i * 1024, L.wvo[i], L.wsrd, L.wso);
    BARRIER();
#pragma unroll
    for (int i = 0; i < 4; ++i) dma16(XBUF + L.xdst + i * 1024, L.xvo[i & 1], L.xsrd, L.xso + i * 10240 + 128);
#pragma unroll
    for (int i = 0; i < 3; ++i) dma16(WRING + (2 + L.whalf) * WSLOT + L.wdst + i * 1024, L.wvo[i], L.wsrd, L.wso + L.wpitch64);
#pragma unroll
    for (int i = 0; i < 4; ++i) dma16(2 * XBUF + L.xdst + i * 1024, L.xvo[i & 1], L.xsrd, L.xso + i * 10240 + 256);
    SB0();
    tag = load_epoch(ws) + 1u;
    {
#pragma unroll
      for (int s = 0; s < 2; ++s) L.xrd[s] = (64 * wm + r16) * 128 + (((4 * s + g) ^ (r16 >> 1)) << 4);
      const int q = r16 >> 2, p = lane & 3;
#pragma unroll
      for (int b = 0; b < 3; ++b) {
        const int ch = (type == 1 ? 5 : 6) * j + 2 * b;
        L.wrd[b] = WRING + (8 * g + q) * 192 + (((ch + (p >> 1)) ^ ((g & 1) << 1)) << 4) + 8 * (p & 1);
      }
    }
    f4 acc[4][6];
#pragma unroll
    for (int i = 0; i < 4; ++i)
#pragma unroll
      for (int b = 0; b < 6; ++b) acc[i][b] = f4{0.f, 0.f, 0.f, 0.f};
    VMCNT(11); BARRIER();
    STAMP(0, 1); STAMPC(0, 6);
    const u32 af = AF_OFF + (u32)(rt * 4 + wm) * (KS2 * 4096u);
    if (type != 1) {
      mainloop<true>(acc, L);
      STAMP(0, 2); STAMPC(0, 7);
      if (type == 0) {
#pragma unroll
        for (int b = 0; b < 3; ++b)
#pragma unroll
          for (int ip = 0; ip < 2; ++ip) {
            f4 h0, h1;
#pragma unroll
            for (int e = 0; e < 4; ++e) {
              h0[e] = sigm(acc[2 * ip][b][e]) * acc[2 * ip][b + 3][e];
              h1[e] = sigm(acc[2 * ip + 1][b][e]) * acc[2 * ip + 1][b + 3][e];
            }
            const u2 v0 = pk4(h0), v1 = pk4(h1);
            BS128((u4{v0[0], v0[1], v1[0], v1[1]}), rs, vo + ip * 1024, af + (6 * c + 3 * j + b) * 2048);
          }
      } else {
#pragma unroll
        for (int b = 0; b < 6; ++b) {
          const int blk = 12 * c + 6 * (b / 3) + 3 * j + b % 3;
          if (blk < 40) {
#pragma unroll
            for (int ip = 0; ip < 2; ++ip) {
              f4 h0, h1;
#pragma unroll
              for (int e = 0; e < 4; ++e) { h0[e] = phi(acc[2 * ip][b][e]); h1[e] = phi(acc[2 * ip + 1][b][e]); }
              const u2 v0 = pk4(h0), v1 = pk4(h1);
              BS128((u4{v0[0], v0[1], v1[0], v1[1]}), rs, vo + ip * 1024, af + (2 * KH + blk) * 2048);
            }
          }
        }
      }
    } else {
      mainloop<false>(acc, L);
      STAMP(0, 2); STAMPC(0, 7);
      constexpr int PA[8] = {0, 0, 1, 1, 1, 2, 2, 0}, PB[8] = {0, 1, 0, 1, 2, 1, 2, 2};
      f4 kv[8];
#pragma unroll
      for (int n = 0; n < 8; ++n) kv[n] = f4{0.f, 0.f, 0.f, 0.f};
#pragma unroll
      for (int s = 0; s < 2; ++s) {
        u4 pk[3], vv[3];
#pragma unroll
        for (int b = 0; b < 3; ++b) {
          f4 p0, p1;
#pragma unroll
          for (int e = 0; e < 4; ++e) { p0[e] = phi(acc[2 * s][b][e]); p1[e] = phi(acc[2 * s + 1][b][e]); }
          const u2 a0 = pk4(p0), a1 = pk4(p1), b0 = pk4(acc[2 * s][3 + b]), b1 = pk4(acc[2 * s + 1][3 + b]);
          pk[b] = u4{a0[0], a0[1], a1[0], a1[1]};
          vv[b] = u4{b0[0], b0[1], b1[0], b1[1]};
        }
        if (r16 == 8) vv[2] = u4{0x3f803f80u, 0x3f803f80u, 0x3f803f80u, 0x3f803f80u};
#pragma unroll
        for (int n = 0; n < 8; ++n) kv[n] = mfma(vv[PB[n]], pk[PA[n]], kv[n]);
      }
      BARRIER();
#pragma unroll
      for (int n = 0; n < 8; ++n) *(LAS f4*)((w * 8 + n) * 1024 + lane * 16) = kv[n];
      LGKM0(); BARRIER();
      const int pa = (0x02211100 >> (4 * w)) & 15, pb = (0x22121010 >> (4 * w)) & 15;
      const int dj = 16 * pa + r16, ej = 16 * pb + 4 * g, hh = dj >= 20;
      const u32 ko = (dj < 40 && ej < 40 && (ej >= 20) == hh) ? (hh * 400 + (dj - 20 * hh) * 20 + (ej - 20 * hh)) * 4 : OOB;
      const u32 zo = (dj < 40 && ej == 40) ? dj * 4 : OOB;
#pragma unroll
      for (int j2 = 0; j2 < 2; ++j2) {
        f4 sum = *(LAS f4*)((j2 * 8 + w) * 1024 + lane * 16);
#pragma unroll
        for (int m = 1; m < 4; ++m) sum += *(LAS f4*)(((2 * m + j2) * 8 + w) * 1024 + lane * 16);
        BS128(sum, rs, ko, KVP_OFF + (rt * 32 + 4 * c + 2 * j2) * 1600);
        __builtin_amdgcn_raw_buffer_store_b32(__builtin_bit_cast(u32, sum[0]), rs, zo, ZP_OFF + (rt * 640 + c * 80 + 40 * j2) * 4, AUX_SC1);
      }
    }
  }
  STAMP(0, 3);
  VMCNT(0); LGKM0(); BARRIER();
  if (tid == 0) flag_store(ws, myflag, tag);
  STAMP(0, 4);

  const int rt = (xcd >> 1) * 8 + (jj >> 2), ct = (xcd & 1) * 4 + (jj & 3), rt8 = rt >> 2;
  const u32 abase = AF_OFF + rt * (KS2 * 4096u), bbase = BF_OFF + ct * (KS2 * 5120u);
  auto ld = [&](Stage& st, u32 sa, u32 sb) {
#pragma unroll
    for (int i = 0; i < 4; ++i) st.a[i] = BL128(rs, vo + i * 1024, sa, LDAUX);
#pragma unroll
    for (int n = 0; n < 5; ++n) st.b[n] = BL128(rs, vo + (n & 3) * 1024, sb + (n >> 2) * 4096, LDAUX);
  };
  auto ldgs = [&](float (&gs)[4]) {
#pragma unroll
    for (int i = 0; i < 4; ++i) gs[i] = __builtin_bit_cast(float, __builtin_amdgcn_raw_buffer_load_b32(rs, r16 * 4 + i * 64, GS_OFF + rt * 256, AUX_SC1));
  };
  auto wait_h = [&](int t0, int t1) {
    const int c0 = t0 / 3, c1 = t1 / 3, cc = c0 + (lane & 7);
    const int idx = lane < 16 ? FL_T + lane : FL_G + rt8 * 18 + (cc < c1 ? cc : c1);
    wait_flags(ws, idx, tag, flag_load(ws, idx));
  };
  f4 acc[4][5];
  float gs[4];
  if (w >= 4) {
    constexpr int DEP = 3;
    Stage st[DEP + 1];
    const int t0 = (w - 4) * NH;
    const u32 sa = abase + t0 * 4096, sb = bbase + t0 * 5120;
    wait_h(t0, t0 + NH - 1);
    STAMP(1, 0); STAMPC(1, 6);
#pragma unroll
    for (int s = 0; s < DEP; ++s) ld(st[s], sa + s * 4096, sb + s * 5120);
#pragma unroll
    for (int i = 0; i < 4; ++i)
#pragma unroll
      for (int n = 0; n < 5; ++n) acc[i][n] = f4{0.f, 0.f, 0.f, 0.f};
    SB0();
#pragma unroll
    for (int s = 0; s < NH; ++s) {
      if (s + DEP < NH) ld(st[(s + DEP) % (DEP + 1)], sa + (s + DEP) * 4096, sb + (s + DEP) * 5120);
      if (s + DEP == NH) ldgs(gs);
      SB0();
      u4 fa[4];
      unpair(st[s % (DEP + 1)].a, fa);
      step_mma(acc, fa, st[s % (DEP + 1)].b);
      SB0();
    }
#pragma unroll
    for (int i = 0; i < 4; ++i) {
      const float f = 1.f - gs[i];
#pragma unroll
      for (int n = 0; n < 5; ++n) acc[i][n] *= f;
    }
    STAMP(1, 3); STAMPC(1, 7);
  } else {
    if (w == 0) {
      {
        const int idx = lane < 8 ? FL_KV + lane * 8 + (rt >> 2) : FL_T + (rt >> 1);
        wait_flags(ws, idx, tag, flag_load(ws, idx));
      }
      u4 pv[8][2][2], wt[5];
      const u32 o0 = r16 * 80 + g * 32, o1 = o0 + 1280;
      const u32 o00 = g < 3 ? o0 : OOB, o01 = g < 2 ? o0 + 16 : OOB, o10 = (g < 3 && r16 < 4) ? o1 : OOB, o11 = (g < 2 && r16 < 4) ? o1 + 16 : OOB;
#pragma unroll
      for (int r = 0; r < 8; ++r) {
        const u32 so = KVP_OFF + (r * 32 + rt) * 1600;
        pv[r][0][0] = BL128(rs, o00, so, AUX_SC1); pv[r][0][1] = BL128(rs, o01, so, AUX_SC1);
        pv[r][1][0] = BL128(rs, o10, so, AUX_SC1); pv[r][1][1] = BL128(rs, o11, so, AUX_SC1);
      }
#pragma unroll
      for (int nb = 0; nb < 5; ++nb) wt[nb] = BL128(rs, vo, WOT_OFF + (rt * 40 + ct * 5 + nb) * 1024, AUX_SC1);
      SB0();
#pragma unroll
      for (int blk = 0; blk < 2; ++blk) {
        f4 s0 = __builtin_bit_cast(f4, pv[0][blk][0]), s1 = __builtin_bit_cast(f4, pv[0][blk][1]);
#pragma unroll
        for (int r = 1; r < 8; ++r) { s0 += __builtin_bit_cast(f4, pv[r][blk][0]); s1 += __builtin_bit_cast(f4, pv[r][blk][1]); }
        const u2 a0 = pk4(s0), a1 = pk4(s1);
        const u4 a = {a0[0], a0[1], a1[0], a1[1]};
        const int q = 5 * rt + 4 * blk + g;
        const u32 dst = (blk == 0 || g == 0) ? (KH + (q >> 3)) * 5120 + ((q & 3) * 16 + r16) * 16 + ((q >> 2) & 1) * 8 : OOB;
#pragma unroll
        for (int nb = 0; nb < 5; ++nb) {
          const f4 m = mfma(a, wt[nb], f4{0.f, 0.f, 0.f, 0.f});
          __builtin_amdgcn_raw_buffer_store_b64(pk4(m), rs, dst, bbase + nb * 1024, AUX_SC1);
        }
      }
      VMCNT(0);
      if (lane == 0) flag_store(ws, FL_M + rt * 8 + ct, tag);
      SB0();
    }
    STAMP(1, 1);
    const int t0 = w == 0 ? KH - 1 : 4 * NH + (w - 1) * NO;
    const u32 sa = abase + t0 * 4096, sb = bbase + t0 * 5120;
    const u32 zl = w * 20480, sl = zl + 512;
    u4 qf[5][4];
    const int midx = FL_M + (8 * w + (lane & 7)) * 8 + ct;
    u32 fl;
    wait_h(t0, w == 0 ? t0 : t0 + NO - 1);
    STAMP(1, 0);
    {
      Stage st[3];
      ldgs(gs);
      ld(st[0], sa, sb);
      ld(st[1], w == 0 ? OOB : sa + 4096, w == 0 ? OOB : sb + 5120);
      ld(st[2], w == 0 ? OOB : sa + 2 * 4096, w == 0 ? OOB : sb + 2 * 5120);
      const int qidx = lane < 16 ? FL_KV + (lane & 7) * 8 + 2 * w + (lane >> 3) : FL_Q + rt8 * 4 + (10 * w + (lane & 1) * 9) / 12;
      fl = flag_load(ws, qidx);
#pragma unroll
      for (int i = 0; i < 4; ++i)
#pragma unroll
        for (int n = 0; n < 5; ++n) acc[i][n] = f4{0.f, 0.f, 0.f, 0.f};
      SB0();
      u4 fa[4];
      unpair(st[0].a, fa); step_mma(acc, fa, st[0].b);
      SB0();
      unpair(st[1].a, fa); step_mma(acc, fa, st[1].b);
      SB0();
      wait_flags(ws, qidx, tag, fl);
      u4 zp[8];
      const u32 zo = (lane < 40 ? lane : 39) * 16;
#pragma unroll
      for (int r = 0; r < 8; ++r) zp[r] = BL128(rs, zo, ZP_OFF + r * 2560 + w * 640, AUX_SC1);
#pragma unroll
      for (int t = 0; t < 2; ++t)
#pragma unroll
        for (int i = 0; i < 4; ++i) qf[t][i] = BL128(rs, vo + i * 1024, abase + (KH + 5 * w + t) * 4096, LDAUX);
      SB0();
      unpair(st[2].a, fa); step_mma(acc, fa, st[2].b);
      SB0();
#pragma unroll
      for (int t = 2; t < 5; ++t)
#pragma unroll
        for (int i = 0; i < 4; ++i) qf[t][i] = BL128(rs, vo + i * 1024, abase + (KH + 5 * w + t) * 4096, LDAUX);
      fl = flag_load(ws, midx);
      SB0();
#pragma unroll
      for (int i = 0; i < 4; ++i) {
        const float f = 1.f - gs[i];
#pragma unroll
        for (int n = 0; n < 5; ++n) acc[i][n] *= f;
      }
      f4 z = __builtin_bit_cast(f4, zp[0]);
#pragma unroll
      for (int r = 1; r < 8; ++r) z += __builtin_bit_cast(f4, zp[r]);
      *(LAS u2*)(zl + lane * 8) = pk4(z);
      SB0();
    }
    STAMP(1, 2);
    {
      f4 den[4];
#pragma unroll
      for (int i = 0; i < 4; ++i) den[i] = f4{0.f, 0.f, 0.f, 0.f};
#pragma unroll
      for (int t = 0; t < 5; ++t) {
        const int q0 = 8 * t + g, q1 = q0 + 4;
        u2 lo = *(LAS u2*)(zl + q0 * 8), hi = *(LAS u2*)(zl + q1 * 8);
        if (q0 / 5 != r16) lo = u2{0u, 0u};
        if (q1 / 5 != r16) hi = u2{0u, 0u};
        const u4 zf = {lo[0], lo[1], hi[0], hi[1]};
        u4 fa[4];
        unpair(qf[t], fa);
#pragma unroll
        for (int i = 0; i < 4; ++i) { qf[t][i] = fa[i]; den[i] = mfma(zf, fa[i], den[i]); }
        SB0();
      }
#pragma unroll
      for (int i = 0; i < 4; ++i) {
        f4 sc;
#pragma unroll
        for (int e = 0; e < 4; ++e) sc[e] = gs[i] * __builtin_amdgcn_rcpf(den[i][e] + 1e-6f);
        *(LAS f4*)(sl + (i * 16 + r16) * 80 + g * 16) = sc;
      }
    }
    SB0();
    wait_flags(ws, midx, tag, fl);
    u4 mf[3][5];
    auto ldm = [&](int t) {
#pragma unroll
      for (int n = 0; n < 5; ++n) mf[t % 3][n] = BL128(rs, vo + (n & 3) * 1024, bbase + (KH + 5 * w + t) * 5120 + (n >> 2) * 4096, LDAUX);
    };
    ldm(0); ldm(1); ldm(2);
    STAMP(1, 3);
#pragma unroll
    for (int t = 0; t < 5; ++t) {
      SB0();
      const int h0 = (8 * t + g) / 5, h1 = (8 * t + 4 + g) / 5;
#pragma unroll
      for (int i = 0; i < 4; ++i) {
        const float s0 = *(LAS float*)(sl + (i * 16 + r16) * 80 + h0 * 4), s1 = *(LAS float*)(sl + (i * 16 + r16) * 80 + h1 * 4);
        const u4 v = qf[t][i];
        qf[t][i] = u4{pk2(bflo(v[0]) * s0, bfhi(v[0]) * s0), pk2(bflo(v[1]) * s0, bfhi(v[1]) * s0),
                      pk2(bflo(v[2]) * s1, bfhi(v[2]) * s1), pk2(bflo(v[3]) * s1, bfhi(v[3]) * s1)};
      }
      step_mma(acc, qf[t], mf[t % 3]);
      SB0();
      if (t + 3 < 5) ldm(t + 3);
    }
  }
  STAMP(1, 4);
#pragma unroll
  for (int i = 0; i < 4; ++i)
#pragma unroll
    for (int n = 0; n < 5; ++n) *(LAS f4*)(w * 20480 + (i * 5 + n) * 1024 + lane * 16) = acc[i][n];
  LGKM0(); BARRIER();
  {
    const int i = w >> 1, n0 = (w & 1) * 3;
    u16* op = out + (rt * 64 + i * 16 + r16) * D_ + ct * 80 + n0 * 16 + 4 * g;
#pragma unroll
    for (int n = 0; n < 3; ++n) {
      if (n0 + n < 5) {
        const u32 a = (i * 5 + n0 + n) * 1024 + lane * 16;
        f4 v = *(LAS f4*)a;
#pragma unroll
        for (int s = 1; s < 8; ++s) v += *(LAS f4*)(a + s * 20480);
        *(u2*)(op + n * 16) = pk4(v);
      }
    }
  }
  if (bid == 0 && w == 1) {
#pragma unroll 1
    for (int k = 0; k < 4; ++k) wait_flags(ws, k * 64 + lane, tag, flag_load(ws, k * 64 + lane));
    if (lane == 0) flag_store(ws, FL_EPOCH, tag);
  }
  STAMP(1, 5);
}

extern "C" void kernel_launch(void* const* d_in, const int* in_sizes, int n_in, void* d_out, int out_size, void* d_ws, size_t ws_size,
                              hipStream_t stream) {
  (void)in_sizes; (void)n_in; (void)out_size; (void)ws_size;
  (void)hipFuncSetAttribute((const void*)fused, hipFuncAttributeMaxDynamicSharedMemorySize, K2_LDS);
  fused<<<256, 512, K2_LDS, stream>>>((const u16*)d_in[0], (const u16*)d_in[1], (const u16*)d_in[2], (const u16*)d_in[4], (const u16*)d_in[5],
                                      (const u16*)d_in[6], (const u16*)d_in[7], (char*)d_ws, (u16*)d_out);
}
```

```cpp
#include <hip/hip_runtime.h>
#include <stdint.h>

typedef unsigned short u16;
typedef unsigned int u32;
typedef unsigned long long u64;
typedef short s4 __attribute__((ext_vector_type(4)));
typedef __bf16 bf8 __attribute__((ext_vector_type(8)));
typedef __bf16 bf2 __attribute__((ext_vector_type(2)));
typedef float f2 __attribute__((ext_vector_type(2)));
typedef float f4 __attribute__((ext_vector_type(4)));
typedef u32 u4 __attribute__((ext_vector_type(4)));
typedef u32 u2 __attribute__((ext_vector_type(2)));
#define LAS __attribute__((address_space(3)))
#define GAS __attribute__((address_space(1)))
#define DEV __device__ __forceinline__
#define SB0() __builtin_amdgcn_sched_barrier(0)

constexpr int S_ = 2048, D_ = 640, F_ = 1728;
constexpr int KS2 = 74, KH = 54;
constexpr u32 AF_OFF = 0, AF_BYTES = 32u * KS2 * 4096u;
constexpr u32 BF_OFF = AF_OFF + AF_BYTES, BF_BYTES = 8u * KS2 * 5120u;
constexpr u32 KVP_OFF = BF_OFF + BF_BYTES, KVP_BYTES = 8u * 32u * 400u * 4u;
constexpr u32 ZP_OFF = KVP_OFF + KVP_BYTES, ZP_BYTES = 8u * 8u * 384u;
constexpr u32 GS_OFF = ZP_OFF + ZP_BYTES, GS_BYTES = 2048u * 8u;
constexpr u32 FLAG_OFF = GS_OFF + GS_BYTES, FLAG_BYTES = 289u * 64u;
constexpr int FL_G = 0, FL_KV = 144, FL_Q = 208, FL_T = 240, FL_M = 256, FL_TA = 272, FL_EPOCH = 288;
constexpr u32 WS_BYTES = FLAG_OFF + FLAG_BYTES;
#define STAMP(k, i) do {} while (0)
#define STAMPC(k, i) do {} while (0)

constexpr u32 XBUF = 32768, WRING = 3 * XBUF, WSLOT = 12288, LDS_BYTES = 163840;
constexpr u32 OOB = 0x40000000u;
constexpr int SC1 = 16;
typedef __amdgpu_buffer_rsrc_t rsrc_t;
#define BL128(rs, vo, so) __builtin_amdgcn_raw_buffer_load_b128(rs, vo, so, SC1)
#define BL64(rs, vo, so) __builtin_amdgcn_raw_buffer_load_b64(rs, vo, so, SC1)
#define BS128(v, rs, off) __builtin_amdgcn_raw_buffer_store_b128(__builtin_bit_cast(u4, v), rs, off, 0, SC1)
#define RLX_AGENT __ATOMIC_RELAXED, __HIP_MEMORY_SCOPE_AGENT

#define RINGSC "sc1"
DEV u4 make_srd(const void* p, u32 bytes) {
  u64 a = (u64)p;
  u4 r = {(u32)a, (u32)(a >> 32) & 0xffffu, bytes, 0x00020000u};
  return r;
}
DEV void dma16(u32 lds, u32 voff, u4 srd, u32 soff) {
  asm volatile("s_mov_b32 m0, %0\n\ts_nop 0\n\tbuffer_load_dwordx4 %1, %2, %3 offen lds" ::"s"(lds), "v"(voff), "s"(srd), "s"(soff) : "memory");
}
template <int OFF> DEV void dma16o(u32 lds, u32 voff, u4 srd, u32 soff) {
  asm volatile("s_mov_b32 m0, %0\n\ts_nop 0\n\tbuffer_load_dwordx4 %1, %2, %3 offen offset:%4 " RINGSC " lds" ::"s"(lds), "v"(voff), "s"(srd), "s"(soff), "i"(OFF) : "memory");
}
DEV void dma16n(int d, u32 lds, u32 voff, u4 srd, u32 soff) {
  if (d == 0) dma16o<0>(lds, voff, srd, soff);
  else if (d == 1) dma16o<1024>(lds, voff, srd, soff);
  else if (d == 2) dma16o<2048>(lds, voff, srd, soff);
  else dma16o<3072>(lds, voff, srd, soff);
}
DEV void dma16c(u32 lds, u32 voff, u4 srd, u32 soff) {
  asm volatile("s_mov_b32 m0, %0\n\ts_nop 0\n\tbuffer_load_dwordx4 %1, %2, %3 offen sc1 lds" ::"s"(lds), "v"(voff), "s"(srd), "s"(soff) : "memory");
}
DEV void dma4c(u32 lds, u32 voff, u4 srd, u32 soff) {
  asm volatile("s_mov_b32 m0, %0\n\ts_nop 0\n\tbuffer_load_dword %1, %2, %3 offen sc1 lds" ::"s"(lds), "v"(voff), "s"(srd), "s"(soff) : "memory");
}
DEV s4 trread(u32 a) { return __builtin_amdgcn_ds_read_tr16_b64_v4i16((LAS s4*)a); }
DEV bf8 cat8(s4 lo, s4 hi) { return __builtin_bit_cast(bf8, __builtin_shufflevector(lo, hi, 0, 1, 2, 3, 4, 5, 6, 7)); }
DEV f4 mfma(bf8 a, bf8 b, f4 c) { return __builtin_amdgcn_mfma_f32_16x16x32_bf16(a, b, c, 0, 0, 0); }
DEV f4 mfma(u4 a, u4 b, f4 c) { return mfma(__builtin_bit_cast(bf8, a), __builtin_bit_cast(bf8, b), c); }
DEV u32 pk2(float a, float b) { f2 v = {a, b}; return __builtin_bit_cast(u32, __builtin_convertvector(v, bf2)); }
DEV u2 pk4(f4 v) { u2 r = {pk2(v[0], v[1]), pk2(v[2], v[3])}; return r; }
DEV u4 pk8(f4 a, f4 b) { const u2 x = pk4(a), y = pk4(b); return u4{x[0], x[1], y[0], y[1]}; }
DEV u32 pick(bool c, u32 a, u32 b) { return c ? a : b; }
DEV float bflo(u32 v) { return __builtin_bit_cast(float, v << 16); }
DEV float bfhi(u32 v) { return __builtin_bit_cast(float, v & 0xffff0000u); }
constexpr float LOG2E = 1.44269504089f;
DEV float phi(float t) {
  return __builtin_fmaxf(t + 1.f, __builtin_amdgcn_fmed3f(__builtin_amdgcn_exp2f(LOG2E * t), 0.f, 1.f)) + 1e-6f;
}
DEV f4 phi4(f4 t) { return f4{phi(t[0]), phi(t[1]), phi(t[2]), phi(t[3])}; }
#define LGKM0() __builtin_amdgcn_s_waitcnt(0xC07F)
#define VMCNT(n) asm volatile("s_waitcnt vmcnt(" #n ")" ::: "memory")
#define BARRIER() do { asm volatile("" ::: "memory"); __builtin_amdgcn_s_barrier(); asm volatile("" ::: "memory"); } while (0)

DEV u32 flag_load(char* ws, int idx) { return __hip_atomic_load((GAS u32*)(ws + FLAG_OFF + idx * 64), RLX_AGENT); }
DEV void flag_store(__amdgpu_buffer_rsrc_t rs, bool who, int idx, u32 v) {
  __builtin_amdgcn_raw_buffer_store_b32(v, rs, who ? 0u : OOB, FLAG_OFF + idx * 64, SC1);
  asm volatile("" ::: "memory");
}
constexpr u32 SPIN_CAP = 1u << 16;
DEV void wait_flags(char* ws, int idx, u32 tag, u32 v) {
  for (u32 spin = 0; !__all(v == tag) && spin < SPIN_CAP; ++spin) {
    __builtin_amdgcn_s_sleep(2);
    v = flag_load(ws, idx);
    asm volatile("" : "+v"(v));
  }
  __builtin_amdgcn_fence(__ATOMIC_ACQUIRE, "wavefront");
}
DEV u32 load_epoch(char* ws) {
  u32 e;
  asm volatile("s_load_dword %0, %1, 0x0\n\ts_waitcnt lgkmcnt(0)" : "=s"(e) : "s"(ws + FLAG_OFF + FL_EPOCH * 64) : "memory");
  return e;
}
DEV void gate_load(rsrc_t rs, int row0, int r16, u2 (&gr)[4]) {
#pragma unroll
  for (int i = 0; i < 4; ++i) gr[i] = BL64(rs, r16 * 8 + i * 128, GS_OFF + row0 * 8);
}
DEV void gate_wait(rsrc_t rs, int row0, int r16, u32 tag, u2 (&gr)[4]) {
  for (u32 spin = 0; !__all(gr[0][1] == tag && gr[1][1] == tag && gr[2][1] == tag && gr[3][1] == tag) && spin < SPIN_CAP; ++spin) {
    __builtin_amdgcn_s_sleep(2);
    asm volatile("" ::: "memory");
    gate_load(rs, row0, r16, gr);
    asm volatile("" : "+v"(gr[0]), "+v"(gr[1]), "+v"(gr[2]), "+v"(gr[3]));
  }
}

struct Loop {
  u4 xsrd, wsrd, bsrd;
  u32 xso, wso, wpitch64, bso;
  u32 xvo[4], wvo[3], vo;
  u32 xdst, wdst, whalf;
  u32 xrd[2], wrd[5];
};
struct St { u4 xsrd, wsrd; u32 xvo[4], wvo[3], xa, wa[5], rw, rx, dw, dx, sw, sx, xinc; };
DEV u32 wrapw(u32 v) { return v >= 5 * WSLOT ? v - 5 * WSLOT : v; }
template <int NB> DEV u32 waddr(const u32 (&wa)[5], int b) { return NB == 6 ? wa[b % 3] + (b / 3) * 6144 : wa[b]; }

template <bool TR, int NB, int H, int MODE, class FN>
DEV void half_step(f4 (&acc)[4][NB], const bf8 (&xc)[4], const bf8 (&wc)[NB], bf8 (&xn)[4], bf8 (&wn)[NB], const Loop& L, St& s, int t, FN fn) {
  constexpr int NA = NB == 6 ? 3 : 5;
  const u32 xa = s.xa;
  u32 wa[5];
#pragma unroll
  for (int b = 0; b < NA; ++b) wa[b] = s.wa[b];
  s4 lo[NB];
#pragma unroll
  for (int k = 0; k < 4 * NB; ++k) {
    const int b = k >> 2, i = k & 3;
    acc[i][b] = TR ? mfma(wc[b], xc[i], acc[i][b]) : mfma(xc[i], wc[b], acc[i][b]);
    SB0();
    if (MODE < 2) {
      if (k < 4) xn[k] = *(LAS bf8*)(xa + k * 2048);
      else if (k < 4 + 2 * NB) {
        const int nb = (k - 4) >> 1;
        const u32 a = waddr<NB>(wa, nb);
        if (k & 1) wn[nb] = cat8(lo[nb], trread(a + 768)); else lo[nb] = trread(a);
      }
    }
    if (MODE == 0) {
      if (k % NB == 1) {
        const int d = k / NB;
        if (H == 0) { if (d < 3) dma16n(d, s.dw, s.wvo[d], s.wsrd, s.sw); }
        else dma16n(d, s.dx, s.xvo[d], s.xsrd, s.sx);
      }
      if (k == 4 * NB - 4) {
        asm volatile("" : "+s"(s.rw), "+s"(s.rx));
        s.rw = wrapw(s.rw + WSLOT);
        if (H == 0) s.rx = s.rx == 2 * XBUF ? 0 : s.rx + XBUF;
        asm volatile("" : "+s"(s.rw), "+s"(s.rx));
      } else if (k == 4 * NB - 3) {
        s.xa = L.xrd[H] + s.rx;
        asm volatile("" : "+v"(s.xa));
#pragma unroll
        for (int b2 = 0; b2 < NA; ++b2) { s.wa[b2] = L.wrd[b2] + s.rw; asm volatile("" : "+v"(s.wa[b2])); }
      } else if (k == 2 * NB + 4) {
        asm volatile("" : "+s"(t));
        const bool sw = t == 7;
        if (H == 1) {
          asm volatile("" : "+s"(s.dw), "+s"(s.sw));
          s.dw = s.dw >= WRING + 3 * WSLOT ? s.dw - 3 * WSLOT : s.dw + 2 * WSLOT;
          s.sw = pick(sw, L.bso + 4096, s.sw + L.wpitch64);
          asm volatile("" : "+s"(s.dw), "+s"(s.sw));
        } else {
          asm volatile("" : "+s"(s.dx), "+s"(s.sx));
          s.dx = s.dx >= 2 * XBUF ? s.dx - 2 * XBUF : s.dx + XBUF;
          s.sx = pick(sw, L.bso, s.sx + s.xinc); s.xinc = pick(sw, 7168, s.xinc);
          asm volatile("" : "+s"(s.dx), "+s"(s.sx), "+s"(s.xinc));
        }
      } else if (k == 2 * NB + 5) {
        asm volatile("" : "+s"(t));
        const bool sw = t == 7;
        u4& srd = H == 1 ? s.wsrd : s.xsrd;
#pragma unroll
        for (int e = 0; e < 3; ++e) srd[e] = pick(sw, L.bsrd[e], srd[e]);
        asm volatile("" : "+s"(srd));
#pragma unroll
        for (int e = 0; e < (H == 1 ? 3 : 4); ++e) {
          u32& v = H == 1 ? s.wvo[e] : s.xvo[e];
          v = pick(sw, L.vo, v);
          asm volatile("" : "+v"(v));
        }
      }
    }
    if (MODE > 0 || k == 4 * NB - 2) fn(MODE > 0 ? k : t);
    SB0();
  }
}

template <bool TR, int NB, class CHK, class PRE, class MID, class FA, class FB, class ST, class ST2>
DEV void mainloop(f4 (&acc)[4][NB], const Loop& L, CHK chk, PRE pre, MID mid, FA fa, FB fb, ST st, ST2 st2) {
  constexpr int NA = NB == 6 ? 3 : 5;
  bf8 x0[4], w0[NB], x1[4], w1[NB];
#pragma unroll
  for (int i = 0; i < 4; ++i)
#pragma unroll
    for (int b = 0; b < NB; ++b) acc[i][b] = f4{0.f, 0.f, 0.f, 0.f};
  St s;
  s.xsrd = L.xsrd; s.wsrd = L.wsrd;
#pragma unroll
  for (int i = 0; i < 4; ++i) s.xvo[i] = L.xvo[i];
#pragma unroll
  for (int i = 0; i < 3; ++i) s.wvo[i] = L.wvo[i];
  s.rw = WSLOT; s.rx = 0; s.xa = L.xrd[1];
#pragma unroll
  for (int b = 0; b < NA; ++b) s.wa[b] = L.wrd[b] + WSLOT;
  s.dw = WRING + (L.whalf ? 0 : 4 * WSLOT) + L.wdst; s.sw = L.wso + 2 * L.wpitch64;
  s.dx = 2 * XBUF + L.xdst; s.sx = L.xso + 2 * 128; s.xinc = 128;
  SB0();
  VMCNT(11); BARRIER();
  st();
#pragma unroll
  for (int i = 0; i < 4; ++i) x0[i] = *(LAS bf8*)(L.xrd[0] + i * 2048);
#pragma unroll
  for (int b = 0; b < NB; ++b) { const u32 a = waddr<NB>(L.wrd, b); w0[b] = cat8(trread(a), trread(a + 768)); }
  auto nop = [](int) {};
#pragma unroll 1
  for (int t = 0; t < 9; ++t) {
    SB0(); LGKM0(); BARRIER(); SB0();
    half_step<TR, NB, 0, 0>(acc, x0, w0, x1, w1, L, s, t, nop);
    SB0(); VMCNT(7); LGKM0(); BARRIER(); SB0();
    half_step<TR, NB, 1, 0>(acc, x1, w1, x0, w0, L, s, t, chk);
  }
  pre();
  SB0(); LGKM0(); BARRIER(); SB0();
  half_step<TR, NB, 0, 1>(acc, x0, w0, x1, w1, L, s, 9, fa);
  mid();
  SB0(); LGKM0(); BARRIER(); SB0();
  half_step<TR, NB, 1, 2>(acc, x1, w1, x0, w0, L, s, 9, fb);
}

DEV constexpr u32 pc_base(int k) {
  return k < 4 ? XBUF + k * 1024 : k < 7 ? WRING + (k - 4) * 1024 : k < 11 ? 2 * XBUF + (k - 7) * 1024 : k < 14 ? WRING + 2 * WSLOT + (k - 11) * 1024
       : k < 18 ? (k - 14) * 1024 : WRING + 4 * WSLOT + (k - 18) * 1024;
}
DEV constexpr int pc_kind(int k) { return (k < 4 || (k >= 7 && k < 11) || (k >= 14 && k < 18)) ? 0 : (k < 18 ? 1 : 2); }

DEV void unpair(const u4 (&p)[4], u4 (&f)[4]) {
#pragma unroll
  for (int ip = 0; ip < 2; ++ip) {
    f[2 * ip] = u4{p[ip][0], p[ip][1], p[2 + ip][0], p[2 + ip][1]};
    f[2 * ip + 1] = u4{p[ip][2], p[ip][3], p[2 + ip][2], p[2 + ip][3]};
  }
}
DEV void step_mma(f4 (&acc)[4][5], const u4 (&a)[4], const u4 (&b)[5]) {
#pragma unroll
  for (int n = 0; n < 5; ++n)
#pragma unroll
    for (int i = 0; i < 4; ++i) acc[i][n] = mfma(b[n], a[i], acc[i][n]);
}

__global__ __launch_bounds__(512, 2) void fused(const u16* __restrict__ x, const u16* __restrict__ wqkv, const u16* __restrict__ wo,
                                                const u16* __restrict__ wg, const u16* __restrict__ wu, const u16* __restrict__ wd,
                                                const u16* __restrict__ gw, char* ws, u16* __restrict__ out) {
  extern __shared__ __attribute__((aligned(16))) char smem[];
  const int tid = threadIdx.x, lane = tid & 63, w = __builtin_amdgcn_readfirstlane(tid >> 6);
  const int bid = blockIdx.x, xcd = bid & 7, jj = bid >> 3;
  const int g = lane >> 4, r16 = lane & 15;
  const rsrc_t rs = __builtin_amdgcn_make_buffer_rsrc(ws, 0, WS_BYTES, 0x00020000);
  const u4 wssrd = make_srd(ws, WS_BYTES);
  const u32 vo = lane * 16;
#define MAP2 1
  const int rt2 = MAP2 == 0 ? (xcd >> 1) * 8 + (jj >> 2) : xcd * 4 + (jj >> 3), ct = MAP2 == 0 ? (xcd & 1) * 4 + (jj & 3) : (jj & 7);
  const u32 abase = AF_OFF + rt2 * (KS2 * 4096u), bbase = BF_OFF + ct * (KS2 * 5120u);
  const int t0 = 7 * w - (w == 7), nh = 7 - (w >= 6), o0 = KH + pick(w < 4, 2 * w, 3 * w - 4), no = 2 + (w >= 4);
  u4 breg[3][5];
  const u32 pcw[3] = {(u32)w * 4096u, (u32)(((w >> 1) & 1) * 4 + (w >> 2) * 2 + (w & 1)) * 3072u, (u32)w * 2048u};
  auto b_dma = [&](int k) { dma16c(pc_base(k) + pcw[pc_kind(k)], vo, wssrd, bbase + t0 * 5120 + k * 1024); };
  auto b_regs = [&](int k0, int k1) {
    const u32 sb = bbase + (t0 + 4) * 5120;
#pragma unroll
    for (int k = k0; k < k1; ++k) breg[k / 5][k % 5] = BL128(rs, vo + (k & 3) * 1024, pick(k >= 10 && nh == 6, OOB, sb + (k >> 2) * 4096));
  };
#define NEP 2
  u32 tag;
  STAMP(0, 0);
  if (jj >= 30) {
    const int tw = (jj - 30) * 8 + xcd;
    u32 so[5];
#pragma unroll
    for (int i = 0; i < 5; ++i) { const int n = i * 64 + lane, row = 4 * w + n / 80, p = n % 80; so[i] = row * 1280 + ((p ^ ((row & 7) << 1)) << 4); }
    const u4 wdsrd = make_srd(wd, F_ * D_ * 2), wosrd = make_srd(wo, D_ * D_ * 2);
    asm volatile("s_nop 4");
#pragma unroll
    for (int s = 0; s < 4; ++s)
      if (s < 3 || tw < 6) {
#pragma unroll
        for (int i = 0; i < 5; ++i) dma16(s * 40960 + w * 5120 + i * 1024, so[i], wdsrd, (tw + 16 * s) * 40960);
      }
    tag = load_epoch(ws) + 1u;
    const int q = r16 >> 2, p = lane & 3;
    VMCNT(0); BARRIER();
#pragma unroll
    for (int s = 0; s < 4; ++s)
      if (s < 3 || tw < 6) {
#pragma unroll
        for (int nb = 0; nb < 5; ++nb) {
          const int ch = 2 * (w * 5 + nb) + (p >> 1), row = 4 * g + q;
          const u32 a = s * 40960 + row * 1280 + ((ch ^ ((row & 7) << 1)) << 4) + 8 * (p & 1);
          BS128(cat8(trread(a), trread(a + 16 * 1280)), rs, vo + BF_OFF + ((w * KS2 + tw + 16 * s) * 5 + nb) * 1024);
        }
      }
    VMCNT(0); LGKM0(); BARRIER();
    flag_store(rs, tid == 0, FL_T + tw, tag);
    if (w == 7) {
      wait_flags(ws, FL_T + r16, tag, flag_load(ws, FL_T + r16));
      flag_store(rs, lane == 0, FL_TA + tw, tag);
    }
    STAMP(0, 1);
    if (w < 5) {
#pragma unroll
      for (int s = 0; s < 2; ++s)
#pragma unroll
        for (int i = 0; i < 5; ++i) dma16(s * 25600 + w * 5120 + i * 1024, so[i], wosrd, (tw * 2 + s) * 25600);
    }
    if (w == 5 || w == 6) dma16(152576 + (w - 5) * 1024, vo, make_srd(gw, D_ * 2), (w - 5) * 1024);
    *(LAS u2*)(147456 + tid * 8) = u2{0u, 0u};
    {
      const int row = tw * 128 + w * 16 + (lane >> 2);
      const u4* xp = (const u4*)(x + row * D_) + (lane & 3);
      u4 xv[20];
#pragma unroll
      for (int c = 0; c < 20; ++c) xv[c] = xp[4 * c];
      VMCNT(0); LGKM0(); BARRIER();
      float a0 = 0.f, a1 = 0.f;
#pragma unroll
      for (int c = 0; c < 20; ++c) {
        const u4 gv = *(LAS u4*)(152576 + (4 * c + (lane & 3)) * 16);
#pragma unroll
        for (int e = 0; e < 4; ++e) { a0 += bflo(xv[c][e]) * bflo(gv[e]); a1 += bfhi(xv[c][e]) * bfhi(gv[e]); }
      }
      a0 += a1;
      a0 += __shfl_xor(a0, 1); a0 += __shfl_xor(a0, 2);
      const float cinv = __builtin_fminf(1.f + __builtin_amdgcn_exp2f(LOG2E * a0), 1e30f);
      __builtin_amdgcn_raw_buffer_store_b64(u2{__builtin_bit_cast(u32, cinv), tag}, rs, pick((lane & 3) == 0, row * 8, OOB), GS_OFF, SC1);
    }
    STAMP(0, 5);
    bf8 wt[2][5];
#pragma unroll
    for (int s = 0; s < 2; ++s)
#pragma unroll
      for (int nb = 0; nb < 5; ++nb) {
        const int ch = 2 * (w * 5 + nb) + (p >> 1);
        s4 v[2];
#pragma unroll
        for (int t = 0; t < 2; ++t) {
          const int e0 = 8 * g + 4 * t, row = (e0 < 20 ? e0 : 0) + q;
          v[t] = trread(s * 25600 + row * 1280 + ((ch ^ ((row & 7) << 1)) << 4) + 8 * (p & 1));
          if (e0 >= 20) v[t] = s4{0, 0, 0, 0};
        }
        wt[s][nb] = cat8(v[0], v[1]);
      }
    LGKM0(); BARRIER();
#pragma unroll
    for (int k = 0; k < 18; ++k) b_dma(k);
    b_regs(0, 6 * NEP);
    if (w < 4) {
      wait_flags(ws, FL_KV + (lane & 7) * 8 + (tw >> 1), tag, flag_load(ws, FL_KV + (lane & 7) * 8 + (tw >> 1)));
      const u32 src = pick(tid < 200, KVP_OFF + tw * 3200 + tid * 16, OOB);
      u4 pv[8];
#pragma unroll
      for (int r = 0; r < 8; ++r) pv[r] = BL128(rs, src, r * 32 * 1600);
      f4 sum = __builtin_bit_cast(f4, pv[0]);
#pragma unroll
      for (int r = 1; r < 8; ++r) sum += __builtin_bit_cast(f4, pv[r]);
      const int hd = tid / 5;
      *(LAS u2*)pick(tid >= 200, 151552 + lane * 8, 147456 + pick(hd >= 20, hd + 12, hd) * 64 + (tid - 5 * hd) * 8) = pk4(sum);
    }
    LGKM0(); BARRIER();
    STAMP(0, 2);
#pragma unroll
    for (int s = 0; s < 2; ++s)
#pragma unroll
      for (int blk = 0; blk < 2; ++blk) {
        const bf8 a = *(LAS bf8*)(147456 + (s * 32 + blk * 16 + r16) * 64 + g * 16);
        const int q4 = 5 * (2 * tw + s) + 4 * blk + g, hf = (q4 >> 2) & 1, ln = (q4 & 3) * 16 + r16;
        const u32 dst = pick(blk == 0 || g == 0, BF_OFF + (w * KS2 + KH + (q4 >> 3)) * 5120u, OOB);
        f4 m[5];
#pragma unroll
        for (int nb = 0; nb < 5; ++nb) m[nb] = mfma(a, wt[s][nb], f4{0.f, 0.f, 0.f, 0.f});
        BS128(pk8(m[0], m[1]), rs, dst + hf * 2048 + ln * 16);
        BS128(pk8(m[2], m[3]), rs, dst + hf * 2048 + 1024 + ln * 16);
        __builtin_amdgcn_raw_buffer_store_b64(pk4(m[4]), rs, dst + 4096 + hf * 512 + ln * 8, 0, SC1);
      }
    STAMP(0, 3);
    VMCNT(0); LGKM0(); BARRIER();
    flag_store(rs, tid == 0, FL_M + tw, tag);
    b_dma(18); b_dma(19);
  } else {
    const int idx = (xcd >> 1) * 30 + jj, rt = (xcd & 1) * 4 + (idx & 3), cq = idx >> 2;
    Loop L;
    L.xsrd = make_srd(x, S_ * D_ * 2);
    L.xso = (rt * 256 + w * 32) * 1280;
#pragma unroll
    for (int i = 0; i < 4; ++i) L.xvo[i] = (lane >> 3) * 1280 + (((lane & 7) ^ ((4 * i + (lane >> 4)) & 7)) << 4) + i * (10240 - 1024);
    L.xdst = w * 4096;
    L.bsrd = wssrd; L.bso = bbase + t0 * 5120; L.vo = vo;
    asm volatile("s_nop 4");
#pragma unroll
    for (int i = 0; i < 4; ++i) dma16n(i, L.xdst, L.xvo[i], L.xsrd, L.xso);
    SB0();
    const int c15 = cq >= 15 ? cq - 15 : cq, up = cq >= 15;
    const int r8 = c15 & 7, h8 = c15 >> 3;
    const int type = (0x02100100 >> (4 * r8)) & 15, ord = (0x40132010 >> (4 * r8)) & 15;
    const int c = up * pick(type == 0, 9, 4 >> ((type - 1) & 1)) + h8 * pick(type == 0, 5, 2 >> ((type - 1) & 1)) + ord;
    const int wm = w >> 1, j = w & 1;
    {
      const int part = w >> 2, wq = w & 3;
      const u32 pitch = type == 0 ? F_ * 2 : 3 * D_ * 2;
      const u32 colbase = pick(type == 0, 96 * c, pick(type == 1, 640 + 640 * part + 80 * c, 160 * c + 80 * part));
      const int nch = type == 0 ? 12 : 10;
      L.wsrd = make_srd(type == 0 ? (part ? wu : wg) : wqkv, D_ * pitch);
      L.wpitch64 = 64 * pitch;
      L.wso = 16 * wq * pitch + colbase * 2;
#pragma unroll
      for (int i = 0; i < 3; ++i) {
        const int n = i * 64 + lane, row = n / 12, ch = (n % 12) ^ (((row >> 3) & 1) << 1);
        L.wvo[i] = pick(ch < nch, row * pitch + (ch << 4) - i * 1024, OOB);
      }
      L.whalf = wq >> 1;
      L.wdst = part * 6144 + (wq & 1) * 3072;
    }
    asm volatile("s_nop 4");
#pragma unroll
    for (int i = 0; i < 3; ++i) dma16n(i, WRING + L.whalf * WSLOT + L.wdst, L.wvo[i], L.wsrd, L.wso);
    BARRIER();
#pragma unroll
    for (int i = 0; i < 4; ++i) dma16n(i, XBUF + L.xdst, L.xvo[i], L.xsrd, L.xso + 128);
#pragma unroll
    for (int i = 0; i < 3; ++i) dma16n(i, WRING + (2 + L.whalf) * WSLOT + L.wdst, L.wvo[i], L.wsrd, L.wso + L.wpitch64);
#pragma unroll
    for (int i = 0; i < 4; ++i) dma16n(i, 2 * XBUF + L.xdst, L.xvo[i], L.xsrd, L.xso + 256);
    SB0();
    tag = load_epoch(ws) + 1u;
    {
#pragma unroll
      for (int s = 0; s < 2; ++s) L.xrd[s] = (64 * wm + r16) * 128 + (((4 * s + g) ^ (r16 >> 1)) << 4);
      const int q = r16 >> 2, p = lane & 3;
#pragma unroll
      for (int b = 0; b < 5; ++b) {
        const int u = 2 * b + (p >> 1);
        const int ch = pick(type == 0, 6 * j + u, pick(type == 1, 5 * j + u - 5 * (u >= 5), u));
        const int part = pick(type == 0, 0, pick(type == 1, u >= 5, j));
        L.wrd[b] = WRING + part * 6144 + (8 * g + q) * 192 + ((ch ^ ((g & 1) << 1)) << 4) + 8 * (p & 1);
      }
    }
    auto st1 = [&]() { STAMP(0, 1); STAMPC(0, 6); };
    auto st2 = [&](int t) {
    };
    const u32 af = AF_OFF + (u32)(rt * 4 + wm) * (KS2 * 4096u) + vo;
    auto chk = [&](int t) {
      u32 tv, ts, cnt;
      asm volatile(
          "s_mov_b32 %2, 0\n\t"
          "s_cmp_eq_u32 %3, 4\n\t"
          "s_cbranch_scc1 2f\n\t"
          "s_cmp_lg_u32 %3, 6\n\t"
          "s_cbranch_scc1 3f\n"
          "1:\n\t"
          "v_mov_b32 %0, %4\n\t"
          "ds_read_b32 %0, %0\n\t"
          "s_waitcnt lgkmcnt(0)\n\t"
          "v_readfirstlane_b32 %1, %0\n\t"
          "s_add_u32 %2, %2, 1\n\t"
          "s_nop 2\n\t"
          "s_cmp_eq_u32 %1, %7\n\t"
          "s_cbranch_scc1 3f\n\t"
          "s_cmp_gt_u32 %2, %8\n\t"
          "s_cbranch_scc1 3f\n\t"
          "s_sleep 2\n"
          "2:\n\t"
          "s_mov_b32 m0, %4\n\t"
          "s_nop 0\n\t"
          "buffer_load_dword off, %5, %6 sc1 lds\n\t"
          "s_cmp_eq_u32 %2, 0\n\t"
          "s_cbranch_scc1 3f\n\t"
          "s_waitcnt vmcnt(0)\n\t"
          "s_branch 1b\n"
          "3:"
          : "=&v"(tv), "=&s"(ts), "=&s"(cnt)
          : "s"(t), "s"(159744 + w * 256), "s"(wssrd), "s"(FLAG_OFF + (FL_TA + (jj & 1) * 8 + xcd) * 64), "s"(tag), "i"(SPIN_CAP)
          : "memory", "scc");
    };
    auto fn_a = [&](int k) { if (k % 6 == 1 && k < 18) b_dma(11 + k / 6); };
    auto fn_b = [&](int k) { if (k % 3 == 1 && k < 18) b_dma(14 + k / 3); };
    auto none = []() {};
    auto nonek = [](int) {};
    int myflag;
    if (type == 0) {
      f4 acc[4][6];
      u2 gr[4];
      const int row0 = rt * 256 + wm * 64;
      auto gate_ask = [&]() {
        asm volatile("buffer_load_dwordx2 %0, %4, %5, %6 offen sc1\n\tbuffer_load_dwordx2 %1, %4, %5, %6 offen offset:128 sc1\n\t"
                     "buffer_load_dwordx2 %2, %4, %5, %6 offen offset:256 sc1\n\tbuffer_load_dwordx2 %3, %4, %5, %6 offen offset:384 sc1"
                     : "=&v"(gr[0]), "=&v"(gr[1]), "=&v"(gr[2]), "=&v"(gr[3]) : "v"((vo & 0xf0) >> 1), "s"(wssrd), "s"(GS_OFF + row0 * 8) : "memory");
      };
      mainloop<true, 6>(acc, L, chk, gate_ask, none, fn_a, fn_b, st1, st2);
      asm volatile("s_waitcnt vmcnt(9)" : "+v"(gr[0]), "+v"(gr[1]), "+v"(gr[2]), "+v"(gr[3])::"memory");
      gate_wait(rs, row0, r16, tag, gr);
      STAMP(0, 2); STAMPC(0, 7);
#pragma unroll
      for (int b = 0; b < 3; ++b)
#pragma unroll
        for (int ip = 0; ip < 2; ++ip) {
          f4 h[2];
#pragma unroll
          for (int o = 0; o < 2; ++o) {
            const float ci = __builtin_bit_cast(float, gr[2 * ip + o][0]);
#pragma unroll
            for (int e = 0; e < 4; ++e)
              h[o][e] = acc[2 * ip + o][b + 3][e] * __builtin_amdgcn_rcpf(__builtin_fmaf(__builtin_amdgcn_exp2f(-LOG2E * acc[2 * ip + o][b][e]), ci, ci));
          }
          BS128(pk8(h[0], h[1]), rs, af + (6 * c + 3 * j + b) * 2048 + ip * 1024);
          b_regs(NEP * (2 * b + ip), NEP * (2 * b + ip + 1));
          SB0();
        }
      myflag = FL_G + rt * 18 + c;
    } else if (type == 2) {
      f4 acc[4][5];
      mainloop<true, 5>(acc, L, chk, none, none, fn_a, fn_b, st1, st2);
      STAMP(0, 2); STAMPC(0, 7);
      u2 gr[4];
      const int row0 = rt * 256 + wm * 64, kidx = FL_KV + (lane & 7) * 8 + 2 * c + j;
      gate_load(rs, row0, r16, gr);
      b_regs(0, 6 * NEP);
      u2 pq[4][6];
#pragma unroll
      for (int i = 0; i < 4; ++i) {
#pragma unroll
        for (int b = 0; b < 5; ++b) { acc[i][b] = phi4(acc[i][b]); pq[i][b] = pk4(acc[i][b]); }
        pq[i][5] = u2{0u, 0u};
      }
      gate_wait(rs, row0, r16, tag, gr);
      wait_flags(ws, kidx, tag, flag_load(ws, kidx));
      u4 zp[8];
#pragma unroll
      for (int r = 0; r < 8; ++r) zp[r] = BL128(rs, pick(lane < 20, lane * 16, OOB), ZP_OFF + (r * 8 + 2 * c + j) * 384);
      f4 z = __builtin_bit_cast(f4, zp[0]);
#pragma unroll
      for (int r = 1; r < 8; ++r) z += __builtin_bit_cast(f4, zp[r]);
      const u2 zb = pk4(z);
      f4 den[4];
#pragma unroll
      for (int i = 0; i < 4; ++i) den[i] = f4{0.f, 0.f, 0.f, 0.f};
#pragma unroll
      for (int s = 0; s < 3; ++s) {
        u4 zf;
#pragma unroll
        for (int o = 0; o < 2; ++o) {
          const int q4 = 8 * s + 4 * o + g, srcl = pick(q4 / 5 == (r16 & 3), q4, 63) * 4;
          zf[2 * o] = __builtin_amdgcn_ds_bpermute(srcl, zb[0]);
          zf[2 * o + 1] = __builtin_amdgcn_ds_bpermute(srcl, zb[1]);
        }
#pragma unroll
        for (int i = 0; i < 4; ++i) den[i] = mfma(zf, u4{pq[i][2 * s][0], pq[i][2 * s][1], pq[i][2 * s + 1][0], pq[i][2 * s + 1][1]}, den[i]);
      }
#pragma unroll
      for (int i = 0; i < 4; ++i) {
        const float gg = 1.f - __builtin_amdgcn_rcpf(__builtin_bit_cast(float, gr[i][0]));
#pragma unroll
        for (int e = 0; e < 4; ++e) den[i][e] = gg * __builtin_amdgcn_rcpf(den[i][e] + 1e-6f);
      }
#pragma unroll
      for (int b = 0; b < 5; ++b)
#pragma unroll
        for (int ip = 0; ip < 2; ++ip) {
          f4 v[2];
#pragma unroll
          for (int o = 0; o < 2; ++o) {
            const f4 d = den[2 * ip + o];
            const float sc = b == 0 ? d[0] : (b == 4 ? d[3] : (g < b ? d[b - 1] : d[b]));
            v[o] = acc[2 * ip + o][b] * sc;
          }
          BS128(pk8(v[0], v[1]), rs, af + (2 * KH + 10 * c + 5 * j + b) * 2048 + ip * 1024);
        }
      myflag = FL_Q + rt * 4 + c;
    } else {
      f4 acc[4][5];
      mainloop<false, 5>(acc, L, chk, none, none, nonek, nonek, st1, st2);
      STAMP(0, 2); STAMPC(0, 7);
      constexpr int PA[8] = {0, 0, 1, 1, 1, 2, 2, 2}, PB[8] = {0, 1, 0, 1, 2, 1, 2, 0};
      f4 kv[8];
#pragma unroll
      for (int n = 0; n < 8; ++n) kv[n] = f4{0.f, 0.f, 0.f, 0.f};
#pragma unroll
      for (int s = 0; s < 2; ++s) {
        u4 pk[3], vv[3];
#pragma unroll
        for (int b = 0; b < 3; ++b) {
          pk[b] = pk8(phi4(acc[2 * s][b]), phi4(acc[2 * s + 1][b]));
          vv[b] = pk8(acc[2 * s][2 + b], acc[2 * s + 1][2 + b]);
        }
        if (r16 == 0) vv[0] = u4{0x3f803f80u, 0x3f803f80u, 0x3f803f80u, 0x3f803f80u};
#pragma unroll
        for (int n = 0; n < 8; ++n) kv[n] = mfma(vv[PB[n]], pk[PA[n]], kv[n]);
      }
#pragma unroll
      for (int n = 0; n < 8; ++n) *(LAS f4*)(w * 8192 + (w >> 2) * 90112 + n * 1024 + lane * 16) = kv[n];
      LGKM0(); BARRIER();
      const int pa = (0x22211100 >> (4 * w)) & 15, pb = (0x02121010 >> (4 * w)) & 15;
      const int dj = 16 * pa + r16, ej = 16 * pb + 4 * g - 8, hh = dj >= 20;
      const u32 ko = pick((dj < 40) & (ej >= 0) & ((ej >= 20) == hh), (hh * 400 + (dj - 20 * hh) * 20 + (ej - 20 * hh)) * 4, OOB);
      const u32 zo = pick((dj < 40) & (ej == -8), dj * 4, OOB);
#pragma unroll
      for (int j2 = 0; j2 < 2; ++j2) {
        f4 sum = *(LAS f4*)((j2 * 8 + w) * 1024 + lane * 16);
#pragma unroll
        for (int m = 1; m < 4; ++m) sum += *(LAS f4*)(((2 * m + j2) * 8 + w) * 1024 + (m >> 1) * 90112 + lane * 16);
        BS128(sum, rs, ko + KVP_OFF + (rt * 32 + 4 * c + 2 * j2) * 1600);
        __builtin_amdgcn_raw_buffer_store_b32(__builtin_bit_cast(u32, sum[0]), rs, zo, ZP_OFF + (rt * 8 + c) * 384 + j2 * 160, SC1);
      }
      myflag = FL_KV + rt * 8 + c;
    }
    STAMP(0, 3);
    VMCNT(0); LGKM0(); BARRIER();
    flag_store(rs, tid == 0, myflag, tag);
    STAMP(0, 5);
    if (type == 1) {
#pragma unroll
      for (int k = 11; k < 20; ++k) b_dma(k);
      b_regs(0, 6 * NEP);
    }
  }
  b_regs(6 * NEP, 15);
  STAMP(0, 4);

  const int rt8 = rt2 >> 2;
  const u32 pcv[3] = {pcw[0] + vo, pcw[1] + vo, pcw[2] + vo};
  auto lda = [&](u4 (&a)[4], int t) {
#pragma unroll
    for (int i = 0; i < 4; ++i) a[i] = BL128(rs, vo + i * 1024, abase + t * 4096);
  };
  auto ldb = [&](u4 (&b)[5], int t) {
#pragma unroll
    for (int n = 0; n < 5; ++n) b[n] = BL128(rs, vo + (n & 3) * 1024, bbase + t * 5120 + (n >> 2) * 4096);
  };
  auto ldm = [&](u4 (&b)[5], int t) {
#pragma unroll
    for (int n = 0; n < 4; ++n) b[n] = BL128(rs, vo + n * 1024, bbase + t * 5120);
    const u2 lo = BL64(rs, lane * 8, bbase + t * 5120 + 4096), hi = BL64(rs, lane * 8 + 512, bbase + t * 5120 + 4096);
    b[4] = u4{lo[0], lo[1], hi[0], hi[1]};
  };
  auto ldsb = [&](u4 (&b)[5], int s) {
#pragma unroll
    for (int n = 0; n < 5; ++n) b[n] = *(LAS u4*)(pc_base(s * 5 + n) + pcv[pc_kind(s * 5 + n)]);
  };
  const int hc = t0 / 3 + (lane & 3), hc1 = (t0 + nh - 1) / 3;
  const int hidx = FL_G + rt8 * 18 + (hc < hc1 ? hc : hc1);
  const int ok0 = (o0 - KH) * 32, ok1 = ok0 + no * 32 - 1, mc = ok0 / 40 + (lane & 3), mc1 = ok1 / 40;
  const int oidx = pick(lane < 4, FL_M + (mc < mc1 ? mc : mc1), FL_Q + rt8 * 4 + pick(lane & 1, ok0, ok1) / 160);
  f4 acc[4][5];
#pragma unroll
  for (int i = 0; i < 4; ++i)
#pragma unroll
    for (int n = 0; n < 5; ++n) acc[i][n] = f4{0.f, 0.f, 0.f, 0.f};
  {
    constexpr int RA = 5;
    u4 ar[RA][4];
    wait_flags(ws, hidx, tag, flag_load(ws, hidx));
    VMCNT(0);
    STAMP(1, 0); STAMPC(1, 6);
    auto kstep = [&](int s) { return s < 3 ? pick(s == 2 && nh == 6, KS2 * 64, t0 + 4 + s) : (s < 7 ? t0 + s - 3 : pick(s == 9 && no == 2, KS2 * 64, o0 + s - 7)); };
#pragma unroll
    for (int s = 0; s < RA - 1; ++s) lda(ar[s], kstep(s));
    u32 fl = 0;
    SB0();
#pragma unroll
    for (int s = 0; s < 10; ++s) {
#define OW 5
      if (s == OW - 2) fl = flag_load(ws, oidx);
      if (s == OW) { wait_flags(ws, oidx, tag, fl); STAMP(1, 2); }
      if (s + RA - 1 < 7) lda(ar[(s + RA - 1) % RA], kstep(s + RA - 1));
      if (s == OW) { lda(ar[7 % RA], kstep(7)); ldm(breg[2], kstep(7)); }
      if (s == (OW > 4 ? OW : 4)) lda(ar[8 % RA], kstep(8));
      if (s == (OW > 5 ? OW : 5)) lda(ar[9 % RA], kstep(9));
      if (s >= 2 && s < 6) ldsb(breg[s & 1], s - 2);
      if (s == 6) ldm(breg[0], kstep(8));
      if (s == 7) ldm(breg[1], kstep(9));
      SB0();
      if ((s != 2 || nh == 7) && (s != 9 || no == 3)) {
        u4 fa[4];
        unpair(ar[s % RA], fa);
        if (s < 7) step_mma(acc, fa, breg[s < 3 ? s : (s - 1) & 1]);
        else {
          const u4 (&mf)[5] = breg[(s - 5) % 3];
          const u4 mp[4] = {mf[0], mf[1], mf[2], mf[3]};
          u4 mu[4];
          unpair(mp, mu);
          const u4 mb[5] = {mu[0], mu[1], mu[2], mu[3], mf[4]};
          step_mma(acc, fa, mb);
        }
      }
      SB0();
    }
    STAMP(1, 3); STAMPC(1, 7);
  }
  STAMP(1, 4);
#pragma unroll
  for (int i = 0; i < 4; ++i)
#pragma unroll
    for (int n = 0; n < 5; ++n) *(LAS f4*)(pc_base(i * 5 + n) + pcv[pc_kind(i * 5 + n)]) = acc[i][n];
  LGKM0(); BARRIER();
  {
    const int i = w >> 1, n0 = (w & 1) * 3;
    u16* op = out + (rt2 * 64 + i * 16 + r16) * D_ + ct * 80 + n0 * 16 + 4 * g;
#pragma unroll
    for (int n = 0; n < 3; ++n) {
      if (n0 + n < 5) {
        const int k = i * 5 + n0 + n;
        const u32 a = pc_base(k) + vo, str = 4096 - 1024 * pc_kind(k);
        f4 v = *(LAS f4*)a;
#pragma unroll
        for (int s = 1; s < 8; ++s) v += *(LAS f4*)(a + s * str);
        *(u2*)(op + n * 16) = pk4(v);
      }
    }
  }
  if (bid == 0 && w == 1) {
    u32 v[4];
#pragma unroll
    for (int k = 0; k < 4; ++k) v[k] = flag_load(ws, k * 64 + lane);
#pragma unroll
    for (int k = 0; k < 4; ++k) wait_flags(ws, k * 64 + lane, tag, v[k]);
    flag_store(rs, lane == 0, FL_EPOCH, tag);
  }
  STAMP(1, 5);
}

extern "C" void kernel_launch(void* const* d_in, const int* in_sizes, int n_in, void* d_out, int out_size, void* d_ws, size_t ws_size,
                              hipStream_t stream) {
  (void)in_sizes; (void)n_in; (void)out_size; (void)ws_size;
  (void)hipFuncSetAttribute((const void*)fused, hipFuncAttributeMaxDynamicSharedMemorySize, LDS_BYTES);
  fused<<<256, 512, LDS_BYTES, stream>>>((const u16*)d_in[0], (const u16*)d_in[1], (const u16*)d_in[2], (const u16*)d_in[4], (const u16*)d_in[5],
                                         (const u16*)d_in[6], (const u16*)d_in[7], (char*)d_ws, (u16*)d_out);
}
```

```cpp
#include <hip/hip_runtime.h>
#include <stdint.h>

typedef unsigned short u16;
typedef unsigned int u32;
typedef unsigned long long u64;
typedef short s4 __attribute__((ext_vector_type(4)));
typedef __bf16 bf8 __attribute__((ext_vector_type(8)));
typedef __bf16 bf2 __attribute__((ext_vector_type(2)));
typedef float f2 __attribute__((ext_vector_type(2)));
typedef float f4 __attribute__((ext_vector_type(4)));
typedef u32 u4 __attribute__((ext_vector_type(4)));
typedef u32 u2 __attribute__((ext_vector_type(2)));
#define LAS __attribute__((address_space(3)))
#define GAS __attribute__((address_space(1)))
#define DEV __device__ __forceinline__
#define SB0() __builtin_amdgcn_sched_barrier(0)

constexpr int S_ = 2048, D_ = 640, F_ = 1728;
constexpr int KS2 = 74, KH = 54;
constexpr u32 AF_OFF = 0, AF_BYTES = 32u * KS2 * 4096u;
constexpr u32 BF_OFF = AF_OFF + AF_BYTES, BF_BYTES = 8u * KS2 * 5120u;
constexpr u32 KVP_OFF = BF_OFF + BF_BYTES, KVP_BYTES = 8u * 16u * 8192u;
constexpr u32 ZP_OFF = KVP_OFF + KVP_BYTES, ZP_BYTES = 8u * 16u * 384u;
constexpr u32 SC_OFF = ZP_OFF + ZP_BYTES, SC_BYTES = 32u * 32u * 256u;
constexpr u32 GS_OFF = SC_OFF + SC_BYTES, GS_BYTES = 2048u * 8u;
constexpr u32 FLAG_OFF = GS_OFF + GS_BYTES, FLAG_BYTES = 561u * 128u;
constexpr int FL_R = 0, FL_G = 256, FL_KV = 400, FL_Q = 464, FL_M = 496, FL_TA = 512, FL_S = 528, FL_EPOCH = 560;
constexpr u32 WS_BYTES = FLAG_OFF + FLAG_BYTES;
#define STAMP(k, i) do {} while (0)
#define STAMPC(k, i) do {} while (0)

constexpr u32 XBUF = 32768, WRING = 3 * XBUF, WSLOT = 12288, WPAD = 8192, LDS_BYTES = 163840;
constexpr u32 RPAD = WRING + 2 * WSLOT, SPARE = WRING + XBUF + 2 * WSLOT;
constexpr u32 OOB = 0x40000000u;
constexpr int SC1 = 16;
typedef __amdgpu_buffer_rsrc_t rsrc_t;
#define BL128(rs, vo, so) __builtin_amdgcn_raw_buffer_load_b128(rs, vo, so, SC1)
#define BL64(rs, vo, so) __builtin_amdgcn_raw_buffer_load_b64(rs, vo, so, SC1)
#define BS128(v, rs, off) __builtin_amdgcn_raw_buffer_store_b128(__builtin_bit_cast(u4, v), rs, off, 0, SC1)
#define RLX_AGENT __ATOMIC_RELAXED, __HIP_MEMORY_SCOPE_AGENT

#define RINGSC "sc1"
DEV u4 make_srd(const void* p, u32 bytes) {
  u64 a = (u64)p;
  u4 r = {(u32)a, (u32)(a >> 32) & 0xffffu, bytes, 0x00020000u};
  return r;
}
DEV void dma16(u32 lds, u32 voff, u4 srd, u32 soff) {
  asm volatile("s_mov_b32 m0, %0\n\ts_nop 0\n\tbuffer_load_dwordx4 %1, %2, %3 offen lds" ::"s"(lds), "v"(voff), "s"(srd), "s"(soff) : "memory");
}
template <int OFF> DEV void dma16o(u32 lds, u32 voff, u4 srd, u32 soff) {
  asm volatile("s_mov_b32 m0, %0\n\ts_nop 0\n\tbuffer_load_dwordx4 %1, %2, %3 offen offset:%4 " RINGSC " lds" ::"s"(lds), "v"(voff), "s"(srd), "s"(soff), "i"(OFF) : "memory");
}
DEV void dma16n(int d, u32 lds, u32 voff, u4 srd, u32 soff) {
  if (d == 0) dma16o<0>(lds, voff, srd, soff);
  else if (d == 1) dma16o<1024>(lds, voff, srd, soff);
  else if (d == 2) dma16o<2048>(lds, voff, srd, soff);
  else dma16o<3072>(lds, voff, srd, soff);
}
DEV void dma16c(u32 lds, u32 voff, u4 srd, u32 soff) {
  asm volatile("s_mov_b32 m0, %0\n\ts_nop 0\n\tbuffer_load_dwordx4 %1, %2, %3 offen sc1 lds" ::"s"(lds), "v"(voff), "s"(srd), "s"(soff) : "memory");
}
DEV void dma4c(u32 lds, u32 voff, u4 srd, u32 soff) {
  asm volatile("s_mov_b32 m0, %0\n\ts_nop 0\n\tbuffer_load_dword %1, %2, %3 offen sc1 lds" ::"s"(lds), "v"(voff), "s"(srd), "s"(soff) : "memory");
}
DEV s4 trread(u32 a) { return __builtin_amdgcn_ds_read_tr16_b64_v4i16((LAS s4*)a); }
DEV bf8 cat8(s4 lo, s4 hi) { return __builtin_bit_cast(bf8, __builtin_shufflevector(lo, hi, 0, 1, 2, 3, 4, 5, 6, 7)); }
DEV f4 mfma(bf8 a, bf8 b, f4 c) { return __builtin_amdgcn_mfma_f32_16x16x32_bf16(a, b, c, 0, 0, 0); }
DEV f4 mfma(u4 a, u4 b, f4 c) { return mfma(__builtin_bit_cast(bf8, a), __builtin_bit_cast(bf8, b), c); }
DEV u32 pk2(float a, float b) { f2 v = {a, b}; return __builtin_bit_cast(u32, __builtin_convertvector(v, bf2)); }
DEV u2 pk4(f4 v) { u2 r = {pk2(v[0], v[1]), pk2(v[2], v[3])}; return r; }
DEV u4 pk8(f4 a, f4 b) { const u2 x = pk4(a), y = pk4(b); return u4{x[0], x[1], y[0], y[1]}; }
DEV u32 pick(bool c, u32 a, u32 b) { return c ? a : b; }
DEV float bflo(u32 v) { return __builtin_bit_cast(float, v << 16); }
DEV float bfhi(u32 v) { return __builtin_bit_cast(float, v & 0xffff0000u); }
constexpr float LOG2E = 1.44269504089f;
DEV void phi_stage(int st, f4 t0, f4 t1, f4 t2, f4 t3, float (&e)[16], float (&p)[16], u32 (&o)[8]) {
  const f4 t[4] = {t0, t1, t2, t3};
#pragma unroll
  for (int q = 0; q < 16; ++q) {
    const float tq = t[q >> 2][q & 3];
    if (st == (q >> 3)) e[q] = tq * LOG2E;
    if (st == 1 + (q >> 2)) e[q] = __builtin_amdgcn_fmed3f(__builtin_amdgcn_exp2f(e[q]), 0.f, 1.f);
    if (st == (q < 4 ? 0 : 1 + (q >> 2))) p[q] = __builtin_amdgcn_fmed3f(tq, 1e-6f, __builtin_inff());
    if (st == 5 + (q >> 3)) { e[q] += p[q]; asm volatile("" : "+v"(e[q])); }
    if (st == 7 && (q & 1)) o[q >> 1] = pk2(e[q - 1], e[q]);
  }
}
DEV void glu_stage(int st, f4 g0, f4 g1, f4 u0, f4 u1, float (&e)[8], u4& o) {
  const f4 gg[2] = {g0, g1}, uu[2] = {u0, u1};
#pragma unroll
  for (int q = 0; q < 8; ++q) {
    if (st == 0) { e[q] = gg[q >> 2][q & 3] * -LOG2E; asm volatile("" : "+v"(e[q])); }
    if (st == 1 + (q >> 2)) e[q] = __builtin_amdgcn_exp2f(e[q]);
    if (st == 3) { e[q] += 1.f; asm volatile("" : "+v"(e[q])); }
    if (st == 4 + (q >> 2)) e[q] = __builtin_amdgcn_rcpf(e[q]);
    if (st == 6) { e[q] *= uu[q >> 2][q & 3]; asm volatile("" : "+v"(e[q])); }
    if (st == 7 && (q & 1)) o[q >> 1] = pk2(e[q - 1], e[q]);
  }
}
#define MEMB() asm volatile("" ::: "memory")
#define VM0C() __builtin_amdgcn_s_waitcnt(0x0F70)
#define SGB(mask, n) __builtin_amdgcn_sched_group_barrier(mask, n, 0)
#define LGKM0() __builtin_amdgcn_s_waitcnt(0xC07F)
#define VMCNT(n) asm volatile("s_waitcnt vmcnt(" #n ")" ::: "memory")
#define BARRIER() do { asm volatile("" ::: "memory"); __builtin_amdgcn_s_barrier(); asm volatile("" ::: "memory"); } while (0)

DEV u32 flag_load(char* ws, int idx) {
  asm volatile("" ::: "memory");
  return __builtin_amdgcn_raw_buffer_load_b32(__builtin_amdgcn_make_buffer_rsrc(ws, 0, WS_BYTES, 0x00020000), idx * 128, FLAG_OFF, SC1);
}
DEV void flag_store(__amdgpu_buffer_rsrc_t rs, u32 id, int idx, u32 v) {
  __builtin_amdgcn_raw_buffer_store_b32(v, rs, id < 32 ? id * 4 : OOB, FLAG_OFF + idx * 128, SC1);
  asm volatile("" ::: "memory");
}
constexpr u32 SPIN_CAP = 1u << 16;
DEV void wait_flags(char* ws, int idx, u32 tag, u32 v) {
#define PSLEEP 2
  for (u32 spin = 0; !__all(v == tag) && spin < SPIN_CAP; ++spin) {
    __builtin_amdgcn_s_sleep(PSLEEP);
    v = flag_load(ws, idx);
    asm volatile("" : "+v"(v));
  }
  asm volatile("" ::: "memory");
}
DEV u32 load_epoch(char* ws) {
  u32 e;
  asm volatile("s_load_dword %0, %1, 0x0\n\ts_waitcnt lgkmcnt(0)" : "=s"(e) : "s"(ws + FLAG_OFF + FL_EPOCH * 128) : "memory");
  return e;
}
DEV void gate_load(rsrc_t rs, int row0, int r16, u2 (&gr)[4]) {
#pragma unroll
  for (int i = 0; i < 4; ++i) gr[i] = BL64(rs, r16 * 8 + i * 128, GS_OFF + row0 * 8);
}
DEV void gate_wait(rsrc_t rs, int row0, int r16, u32 tag, u2 (&gr)[4]) {
  for (u32 spin = 0; !__all(gr[0][1] == tag && gr[1][1] == tag && gr[2][1] == tag && gr[3][1] == tag) && spin < SPIN_CAP; ++spin) {
    __builtin_amdgcn_s_sleep(2);
    asm volatile("" ::: "memory");
    gate_load(rs, row0, r16, gr);
    asm volatile("" : "+v"(gr[0]), "+v"(gr[1]), "+v"(gr[2]), "+v"(gr[3]));
  }
}

struct Loop {
  u4 xsrd, wsrd;
  u32 xso, wso, wpitch64;
  u32 xvo[4], wvo[3];
  u32 xdst, wdst;
  u32 xrd[2], wrd[5];
};
struct St { u32 xa, wa[5], rx, dw, dx, sw, sx; };
template <int NB> DEV u32 waddr(const u32 (&wa)[5], int b) { return NB == 6 ? wa[b % 3] + (b / 3) * 6144 : wa[b]; }

template <bool TR, int NB, int H, int MODE, class FN>
DEV void half_step(f4 (&acc)[4][NB], const bf8 (&xc)[4], const bf8 (&wc)[NB], bf8 (&xn)[4], bf8 (&wn)[NB], const Loop& L, St& s, FN fn) {
  constexpr int NA = NB == 6 ? 3 : 5;
  s4 lo[NB];
#pragma unroll
  for (int k = 0; k < 4 * NB; ++k) {
    const int b = k >> 2, i = k & 3;
    acc[i][b] = TR ? mfma(wc[b], xc[i], acc[i][b]) : mfma(xc[i], wc[b], acc[i][b]);
    SB0();
    if (MODE < 2) {
      if (k < 4) xn[k] = *(LAS bf8*)(s.xa + k * 2048);
      else if (k < 4 + 2 * NB) {
        const int nb = (k - 4) >> 1;
        const u32 a = waddr<NB>(s.wa, nb) + (H == 0 ? WSLOT : 0);
        if (k & 1) wn[nb] = cat8(lo[nb], trread(a + 768)); else lo[nb] = trread(a);
      }
      if (k == 4) {
        if (H == 0) {
          asm volatile("" : "+s"(s.rx));
          s.rx = s.rx == 2 * XBUF ? 0 : s.rx + XBUF;
        }
        s.xa = L.xrd[H] + s.rx;
        asm volatile("" : "+v"(s.xa));
      }
      if (H == 0 && k >= 4 + 2 * NB && k < 4 + 2 * NB + NA) {
        s.wa[k - 4 - 2 * NB] ^= WRING ^ (WRING + 2 * WSLOT + WPAD);
        asm volatile("" : "+v"(s.wa[k - 4 - 2 * NB]));
      }
    }
    if (MODE == 0) {
      if (k % NB == 1) {
        const int d = k / NB;
        if (H == 1) { if (d < 3) dma16n(d, s.dw, L.wvo[d], L.wsrd, s.sw); }
        else dma16n(d, s.dx, L.xvo[d], L.xsrd, s.sx);
      }
      if (k == 2) {
        if (H == 0) {
          asm volatile("" : "+s"(s.dw), "+s"(s.sw));
          s.dw ^= WRING ^ (WRING + 2 * WSLOT + WPAD); s.sw += L.wpitch64;
          asm volatile("" : "+s"(s.dw), "+s"(s.sw));
        } else {
          asm volatile("" : "+s"(s.dx), "+s"(s.sx));
          s.dx = s.dx >= 2 * XBUF ? s.dx - 2 * XBUF : s.dx + XBUF; s.sx += 128;
          asm volatile("" : "+s"(s.dx), "+s"(s.sx));
        }
      }
    }
    fn(k);
    SB0();
  }
}

template <bool TR, int NB, class ASK, class LOOK, class F0, class F1, class ST, class ST2>
DEV void mainloop(f4 (&acc)[4][NB], bf8 (&xf)[2][4], bf8 (&wf)[2][NB], const Loop& L, ASK ask, LOOK look, F0 f8a, F1 f8b, ST st, ST2 st2) {
  constexpr int NA = NB == 6 ? 3 : 5;
  bf8 x1[4], w1[NB];
#pragma unroll
  for (int i = 0; i < 4; ++i)
#pragma unroll
    for (int b = 0; b < NB; ++b) { acc[i][b] = f4{0.f, 0.f, 0.f, 0.f}; asm volatile("" : "+v"(acc[i][b])); }
  St s;
  s.rx = 0; s.xa = L.xrd[1];
#pragma unroll
  for (int b = 0; b < NA; ++b) s.wa[b] = L.wrd[b];
  s.dw = L.wdst ^ (WRING ^ (WRING + 2 * WSLOT + WPAD)); s.sw = L.wso + L.wpitch64;
  s.dx = 2 * XBUF + L.xdst; s.sx = L.xso + 2 * 128;
  SB0();
  VMCNT(9);
  BARRIER();
  st();
#pragma unroll
  for (int i = 0; i < 4; ++i) xf[0][i] = *(LAS bf8*)(L.xrd[0] + i * 2048);
#pragma unroll
  for (int b = 0; b < NB; ++b) { const u32 a = waddr<NB>(L.wrd, b); wf[0][b] = cat8(trread(a), trread(a + 768)); }
  auto nop = [](int) {};
#pragma unroll 1
  for (int t = 0; t < 8; ++t) {
    SB0(); LGKM0(); SB0();
    half_step<TR, NB, 0, 0>(acc, xf[0], wf[0], x1, w1, L, s, nop);
    SB0(); VMCNT(4); LGKM0(); BARRIER(); SB0();
    half_step<TR, NB, 1, 0>(acc, x1, w1, xf[0], wf[0], L, s, [&](int k) { ask(t, k); });
  }
  look();
  SB0(); LGKM0(); SB0();
  half_step<TR, NB, 0, 1>(acc, xf[0], wf[0], x1, w1, L, s, f8a);
  SB0();
  VMCNT(4); LGKM0(); BARRIER(); SB0();
  s4 lo[NB];
  half_step<TR, NB, 1, 1>(acc, x1, w1, xf[0], wf[0], L, s, [&](int k) {
    const int r = k - 2;
    if (r >= 0 && r < 2 * NB) {
      const u32 a = waddr<NB>(s.wa, r >> 1) + WSLOT;
      if (r & 1) wf[1][r >> 1] = cat8(lo[r >> 1], trread(a + 768)); else lo[r >> 1] = trread(a);
    } else if (r >= 2 * NB && r < 2 * NB + 4) xf[1][r - 2 * NB] = *(LAS bf8*)(L.xrd[1] + (r - 2 * NB) * 2048);
    f8b(k);
  });
  SB0(); LGKM0(); BARRIER(); SB0();
}

DEV constexpr u32 pc_base(int k) { return (k < 4 ? XBUF : k < 8 ? WRING : k < 12 ? 2 * XBUF : k < 16 ? WRING + XBUF : 0) + (k & 3) * 1024; }

DEV void unpair1(u4 (&p)[4], int q) {
  const int ip = q >> 1, e = q & 1;
  u32 x = p[ip][2 + e], y = p[2 + ip][e];
  asm volatile("v_swap_b32 %0, %1" : "+v"(x), "+v"(y));
  p[ip][2 + e] = x; p[2 + ip][e] = y;
}
DEV void mma_in(f4& c, u4 a, u4 b, bool first) {
  if (first) asm volatile("v_mfma_f32_16x16x32_bf16 %0, %1, %2, 0" : "=&v"(c) : "v"(a), "v"(b));
  else asm volatile("v_mfma_f32_16x16x32_bf16 %0, %1, %2, %0" : "+v"(c) : "v"(a), "v"(b));
}
DEV constexpr int opix(int i) { return 2 * (i & 1) + (i >> 1); }
template <int V> struct IC { static constexpr int v = V; };

__global__ __launch_bounds__(512, 2) void fused(const u16* __restrict__ x, const u16* __restrict__ wqkv, const u16* __restrict__ wo,
                                                const u16* __restrict__ wg, const u16* __restrict__ wu, const u16* __restrict__ wd,
                                                const u16* __restrict__ gw, char* ws, u16* __restrict__ out) {
  extern __shared__ __attribute__((aligned(16))) char smem[];
  const int tid = threadIdx.x, lane = tid & 63, w = __builtin_amdgcn_readfirstlane(tid >> 6);
  const int bid = blockIdx.x, xcd = bid & 7, jj = bid >> 3;
  const int g = lane >> 4, r16 = lane & 15;
  const rsrc_t rs = __builtin_amdgcn_make_buffer_rsrc(ws, 0, WS_BYTES, 0x00020000);
  const u4 wssrd = make_srd(ws, WS_BYTES);
  const u32 vo = lane * 16;
#define MAP2 1
  const int rt2 = MAP2 == 0 ? (xcd >> 1) * 8 + (jj >> 2) : xcd * 4 + (jj >> 3), ct = MAP2 == 0 ? (xcd & 1) * 4 + (jj & 3) : (jj & 7);
  const u32 abase = AF_OFF + rt2 * (KS2 * 4096u), bbase = BF_OFF + ct * (KS2 * 5120u);
  const int t0 = 7 * w - (w == 7), nh = 7 - (w >= 6), o0 = KH + pick(w < 4, 3 * w, 2 * w + 4), no = 2 + (w < 4);
  u4 breg[3][5];
  auto b_dma = [&](int k) { dma16n(k & 3, pc_base(k & ~3) + w * 4096, vo, wssrd, bbase + t0 * 5120 + (k & ~3) * 1024); };
  auto b_regs = [&](int k0, int k1) {
    const u32 sb = bbase + (t0 + 4) * 5120;
#pragma unroll
    for (int k = k0; k < k1; ++k) breg[k / 5][k % 5] = BL128(rs, vo + (k & 3) * 1024, pick(k >= 10 && nh == 6, OOB, sb + (k >> 2) * 4096));
  };
#define NBR 15
  const u4 wdsrd = make_srd(wd, F_ * D_ * 2);
  auto r_in = [&](int u, u32 lds) {
    const int row = 4 * w + g;
    dma16(lds + w * 1024, row * 1280 + ((r16 ^ ((row & 7) << 1)) << 4), wdsrd, (u / 5) * 40960 + (u % 5) * 256);
  };
  auto r_out = [&](int u, u32 lds) {
    const int row = 4 * g + (r16 >> 2), n40 = (u % 5) * 8 + w;
    const u32 a = lds + row * 256 + (((2 * w + ((lane >> 1) & 1)) ^ ((row & 7) << 1)) << 4) + 8 * (lane & 1);
    BS128(cat8(trread(a), trread(a + 4096)), rs, vo + BF_OFF + (((n40 / 5) * KS2 + u / 5) * 5 + n40 % 5) * 1024);
  };
  const int hc = t0 / 3 + (lane & 3), hc1 = (t0 + nh - 1) / 3, hidx = FL_G + (rt2 >> 2) * 18 + (hc < hc1 ? hc : hc1);
  u32 tag, hv0;
  STAMP(0, 0);
  if (jj >= 30) {
    const int tw = (jj - 30) * 8 + xcd;
    u32 so[5];
#pragma unroll
    for (int i = 0; i < 5; ++i) { const int n = i * 64 + lane, row = 4 * w + n / 80, p = n % 80; so[i] = row * 1280 + ((p ^ ((row & 7) << 1)) << 4); }
    const u4 wosrd = make_srd(wo, D_ * D_ * 2);
    const int u0 = 240 + 2 * tw;
    asm volatile("s_nop 4");
    if (tw < 15) { r_in(u0, RPAD); r_in(u0 + 1, SPARE); }
    if (w < 5) {
#pragma unroll
      for (int s = 0; s < 2; ++s)
#pragma unroll
        for (int i = 0; i < 5; ++i) dma16(s * 25600 + w * 5120 + i * 1024, so[i], wosrd, (tw * 2 + s) * 25600);
    }
    tag = load_epoch(ws) + 1u;
    const int q = r16 >> 2, p = lane & 3;
    VMCNT(0); BARRIER();
    if (tw < 15) { r_out(u0, RPAD); r_out(u0 + 1, SPARE); }
    VM0C(); VMCNT(0); LGKM0(); BARRIER();
    flag_store(rs, tid, FL_R + bid, tag);
    STAMP(0, 1);
    u4 xv[16], xt[4], gv[2];
    {
      const u4* xp = (const u4*)(x + (tw * 128 + w * 16) * D_);
      gv[0] = ((const u4*)gw)[lane]; gv[1] = ((const u4*)gw)[64 + r16];
#pragma unroll
      for (int r = 0; r < 16; ++r) xv[r] = xp[r * 80 + lane];
#pragma unroll
      for (int i = 0; i < 4; ++i) xt[i] = xp[(4 * i + g) * 80 + 64 + r16];
    }
    {
      auto dot8 = [](u4 a, u4 b) {
        float s0 = 0.f;
#pragma unroll
        for (int e = 0; e < 4; ++e) { const u32 ae = a[e], be = b[e]; s0 = __builtin_amdgcn_fdot2_f32_bf16(__builtin_bit_cast(bf2, ae), __builtin_bit_cast(bf2, be), s0, false); }
        return s0;
      };
      float v[16];
#pragma unroll
      for (int r = 0; r < 16; ++r) v[r] = dot8(xv[r], gv[0]);
#pragma unroll
      for (int i = 0; i < 4; ++i) {
        const float tl = dot8(xt[i], gv[1]);
#pragma unroll
        for (int e = 0; e < 4; ++e) v[4 * i + e] += g == e ? tl : 0.f;
      }
#pragma unroll
      for (int b = 0; b < 4; ++b) {
        const bool hi = (lane >> b) & 1;
#pragma unroll
        for (int r = 0; r < (8 >> b); ++r) {
          const float keep = hi ? v[2 * r + 1] : v[2 * r], send = hi ? v[2 * r] : v[2 * r + 1];
          v[r] = keep + __shfl_xor(send, 1 << b);
        }
      }
      float a0 = v[0];
      a0 += __shfl_xor(a0, 16); a0 += __shfl_xor(a0, 32);
      const float gate = __builtin_amdgcn_exp2f(LOG2E * __builtin_amdgcn_fmed3f(a0, -60.f, 60.f));
      __builtin_amdgcn_raw_buffer_store_b64(u2{__builtin_bit_cast(u32, gate), tag}, rs, pick(lane < 16, lane * 8, OOB), GS_OFF + (tw * 128 + w * 16) * 8, SC1);
    }
    STAMP(0, 5);
    if (w == 0) {
      u32 v[4];
#pragma unroll
      for (int k = 0; k < 4; ++k) v[k] = flag_load(ws, FL_R + k * 64 + lane);
#pragma unroll
      for (int k = 0; k < 4; ++k) wait_flags(ws, FL_R + k * 64 + lane, tag, v[k]);
      flag_store(rs, lane, FL_TA + tw, tag);
      flag_store(rs, lane, pick(tw == 0, FL_EPOCH, OOB / 128), tag);
      STAMP(1, 1);
    }
    bf8 wt[2][5];
#pragma unroll
    for (int s = 0; s < 2; ++s)
#pragma unroll
      for (int nb = 0; nb < 5; ++nb) {
        const int ch = 2 * (w * 5 + nb) + (p >> 1);
        s4 v[2];
#pragma unroll
        for (int t = 0; t < 2; ++t) {
          const int e0 = 8 * g + 4 * t, row = (e0 < 20 ? e0 : 0) + q;
          v[t] = trread(s * 25600 + row * 1280 + ((ch ^ ((row & 7) << 1)) << 4) + 8 * (p & 1));
          if (e0 >= 20) v[t] = s4{0, 0, 0, 0};
        }
        wt[s][nb] = cat8(v[0], v[1]);
      }
    LGKM0(); BARRIER();
    *(LAS u2*)(tid * 8) = u2{0u, 0u};
    LGKM0(); BARRIER();
#pragma unroll
    for (int k = 0; k < 16; ++k) b_dma(k);
    if (w >= 2) { b_dma(16); b_dma(17); b_dma(18); b_dma(19); }
    b_regs(0, 15);
    const int dj = 16 * ((0x22211100 >> (4 * w)) & 15) + r16, ej = 16 * ((0x02121010 >> (4 * w)) & 15) + 4 * g - 8, hh = dj >= 20;
    u32 kdst = pick((dj < 40) & (ej >= 0) & ((ej >= 20) == hh), (dj + 12 * hh) * 64 + (ej - 20 * hh) * 2, 4096 + lane * 8), mdst[4];
#pragma unroll
    for (int sb = 0; sb < 4; ++sb) {
      const int q4 = 5 * (2 * tw + (sb >> 1)) + 4 * (sb & 1) + g;
      mdst[sb] = pick((sb & 1) == 0 || g == 0, BF_OFF + (w * KS2 + KH + (q4 >> 3)) * 5120u + ((q4 >> 2) & 1) * 512 + ((q4 & 3) * 16 + r16) * 8, OOB);
      asm volatile("" : "+v"(mdst[sb]));
    }
    asm volatile("" : "+v"(kdst));
    VM0C();
    if (w == 0) wait_flags(ws, FL_KV + (lane & 7) * 8 + (tw >> 1), tag, flag_load(ws, FL_KV + (lane & 7) * 8 + (tw >> 1)));
    BARRIER();
    STAMP(1, 6);
    {
      u4 pv[8];
#pragma unroll
      for (int r = 0; r < 8; ++r) pv[r] = BL128(rs, vo, pick(w < 7, KVP_OFF + ((r * 16 + tw) * 8 + w) * 1024, OOB));
      f4 sum = __builtin_bit_cast(f4, pv[0]);
#pragma unroll
      for (int r = 1; r < 8; ++r) sum += __builtin_bit_cast(f4, pv[r]);
      *(LAS u2*)kdst = pk4(sum);
    }
    LGKM0(); BARRIER();
    STAMP(0, 2);
    bf8 ka[4];
#pragma unroll
    for (int sb = 0; sb < 4; ++sb) ka[sb] = *(LAS bf8*)((sb * 16 + r16) * 64 + g * 16);
#pragma unroll
    for (int sb = 0; sb < 4; ++sb) {
      f4 m[5];
#pragma unroll
      for (int nb = 0; nb < 5; ++nb) m[nb] = mfma(ka[sb], wt[sb >> 1][nb], f4{0.f, 0.f, 0.f, 0.f});
      const u32 lo = mdst[sb] & 1023, d16 = mdst[sb] - lo + (lo >> 9) * 2048 + (lo & 511) * 2;
      BS128(pk8(m[0], m[1]), rs, d16);
      BS128(pk8(m[2], m[3]), rs, d16 + 1024);
      __builtin_amdgcn_raw_buffer_store_b64(pk4(m[4]), rs, mdst[sb] + 4096, 0, SC1);
    }
    STAMP(0, 3);
    hv0 = flag_load(ws, hidx);
    VM0C(); VMCNT(0); BARRIER();
    flag_store(rs, tid, FL_M + tw, tag);
    STAMP(1, 7);
    if (w < 2) { b_dma(16); b_dma(17); b_dma(18); b_dma(19); }
  } else {
    const int idx = (xcd >> 1) * 30 + jj, rt = (xcd & 1) * 4 + (idx & 3), cq = idx >> 2;
    Loop L;
    L.xsrd = make_srd(x, S_ * D_ * 2);
    L.xso = (rt * 256 + w * 32) * 1280;
#pragma unroll
    for (int i = 0; i < 4; ++i) L.xvo[i] = (lane >> 3) * 1280 + (((lane & 7) ^ ((4 * i + (lane >> 4)) & 7)) << 4) + i * (10240 - 1024);
    L.xdst = w * 4096;
    asm volatile("s_nop 4");
#pragma unroll
    for (int i = 0; i < 4; ++i) dma16n(i, L.xdst, L.xvo[i], L.xsrd, L.xso);
    SB0();
    const int c15 = cq >= 15 ? cq - 15 : cq, up = cq >= 15;
    const int r8 = c15 & 7, h8 = c15 >> 3;
    const int type = (0x02100100 >> (4 * r8)) & 15, ord = (0x40132010 >> (4 * r8)) & 15;
    const int c = up * pick(type == 0, 9, 4 >> ((type - 1) & 1)) + h8 * pick(type == 0, 5, 2 >> ((type - 1) & 1)) + ord;
    const int wm = w >> 1, j = w & 1;
    {
      const int part = w >> 2, wq = w & 3;
      const u32 pitch = type == 0 ? F_ * 2 : 3 * D_ * 2;
      const u32 colbase = pick(type == 0, 96 * c, pick(type == 1, 640 + 640 * part + 80 * c, 160 * c + 80 * part));
      const int nch = type == 0 ? 12 : 10;
      L.wsrd = make_srd(type == 0 ? (part ? wu : wg) : wqkv, D_ * pitch);
      L.wpitch64 = 64 * pitch;
      L.wso = 16 * wq * pitch + colbase * 2;
#pragma unroll
      for (int i = 0; i < 3; ++i) {
        const int n = i * 64 + lane, row = n / 12, ch = (n % 12) ^ (((row >> 3) & 1) << 1);
        L.wvo[i] = pick(ch < nch, row * pitch + (ch << 4) - i * 1024, OOB);
      }
      L.wdst = WRING + (wq >> 1) * WSLOT + part * 6144 + (wq & 1) * 3072;
    }
    asm volatile("s_nop 4");
#pragma unroll
    for (int i = 0; i < 3; ++i) dma16n(i, L.wdst, L.wvo[i], L.wsrd, L.wso);
    BARRIER();
#pragma unroll
    for (int i = 0; i < 4; ++i) dma16n(i, XBUF + L.xdst, L.xvo[i], L.xsrd, L.xso + 128);
#pragma unroll
    for (int i = 0; i < 3; ++i) dma16n(i, L.wdst + XBUF, L.wvo[i], L.wsrd, L.wso + L.wpitch64);
    r_in(bid, RPAD);
    SB0();
    {
#pragma unroll
      for (int s = 0; s < 2; ++s) L.xrd[s] = (64 * wm + r16) * 128 + (((4 * s + g) ^ (r16 >> 1)) << 4);
      const int q = r16 >> 2, p = lane & 3;
#pragma unroll
      for (int b = 0; b < 5; ++b) {
        const int u = 2 * b + (p >> 1);
        const int ch = pick(type == 0, 6 * j + u, pick(type == 1, 5 * j + u - 5 * (u >= 5), u));
        const int part = pick(type == 0, 0, pick(type == 1, u >= 5, j));
        L.wrd[b] = WRING + part * 6144 + (8 * g + q) * 192 + ((ch ^ ((g & 1) << 1)) << 4) + 8 * (p & 1);
      }
    }
    const u32 tch = (w & 3) < 2 ? ((128 + rt * 64 + (w & 1) * 32 + (lane >> 1)) * (L.wpitch64 >> 6) + (lane & 1) * 128 - 16 * (w & 3) * (L.wpitch64 >> 6))
                                : pick(lane < 60 && cq * 60 + lane < 1792, ((cq * 60 + lane) / 7) * 1280 + (3 + (cq * 60 + lane) % 7) * 128, OOB);
    {
      const bool isw = (w & 3) < 2;
      u4 tsrd = isw ? L.wsrd : L.xsrd;
      tsrd[2] = pick(isw || w == 2, tsrd[2], 0u);
      dma4c(SPARE + w * 256, tch, tsrd, pick(isw, L.wso, rt * 256 * 1280));
    }
    tag = load_epoch(ws) + 1u;
    auto st1 = [&]() { STAMP(0, 1); STAMPC(0, 6); };
    auto st2 = [&](int t) {
    };
    const u32 af = AF_OFF + (u32)(rt * 4 + wm) * (KS2 * 4096u) + vo;
    const u32 tadr = FLAG_OFF + (FL_TA + (jj & 1) * 8 + xcd) * 128;
    u32 rlds;
    {
      const int row = 4 * g + (r16 >> 2);
      rlds = RPAD + row * 256 + (((2 * w + ((lane >> 1) & 1)) ^ ((row & 7) << 1)) << 4) + 8 * (lane & 1);
      asm volatile("" : "+v"(rlds));
    }
    const int n40 = (bid % 5) * 8 + w;
    const u32 rdst = BF_OFF + (((n40 / 5) * KS2 + bid / 5) * 5 + n40 % 5) * 1024;
    u4 fsrd = wssrd;
    fsrd[2] = pick(w == 0, FLAG_OFF + (FL_R + bid + 1) * 128, 0u);
    auto ask = [&](int t, int k) {
      if (k == 0) {
        asm volatile(
            "s_bitcmp1_b32 0x43, %0\n\t"
            "s_cbranch_scc0 3f\n\t"
            "s_cmp_eq_u32 %0, 1\n\t"
            "s_cbranch_scc1 1f\n\t"
            "s_cmp_eq_u32 %0, 6\n\t"
            "s_cbranch_scc1 2f\n\t"
            "ds_read_b64_tr_b16 v[252:253], %1\n\t"
            "ds_read_b64_tr_b16 v[254:255], %1 offset:4096\n\t"
            "s_waitcnt lgkmcnt(0)\n\t"
            "buffer_store_dwordx4 v[252:255], %2, %3, %4 offen sc1\n\t"
            "s_nop 1\n\t"
            "s_branch 3f\n"
            "1:\n\t"
            "v_mov_b32 v252, %7\n\t"
            "v_lshrrev_b32 v253, 2, %2\n\t"
            "buffer_store_dword v252, v253, %5, %6 offen sc1\n\t"
            "s_branch 3f\n"
            "2:\n\t"
            "s_mov_b32 m0, %8\n\t"
            "s_nop 0\n\t"
            "buffer_load_dword off, %3, %9 sc1 lds\n"
            "3:"
            :
            : "s"(t), "v"(rlds), "v"(vo), "s"(wssrd), "s"(rdst), "s"(fsrd), "s"(FLAG_OFF + (FL_R + bid) * 128), "s"(tag), "s"(SPARE + w * 256), "s"(tadr)
            : "memory", "scc", "v252", "v253", "v254", "v255");
      }
    };
    auto look = [&]() {
      u32 v = *(LAS u32*)(SPARE + w * 256);
      for (u32 spin = 0; __builtin_amdgcn_readfirstlane(v) != tag && spin < SPIN_CAP; ++spin) {
        __builtin_amdgcn_s_sleep(2);
        asm volatile("buffer_load_dword %0, off, %1, %2 sc1\n\ts_waitcnt vmcnt(0)" : "=v"(v) : "s"(wssrd), "s"(tadr) : "memory");
      }
    };
    auto f8a = [&](int k) { if (k % 5 == 1 && k < 20) b_dma(k / 5); };
    auto f8b = [&](int k) { if (k % 2 == 1 && k < 16) b_dma(4 + k / 2); };
    bf8 xf[2][4];
    if (type == 0) {
      f4 acc[4][6];
      bf8 wf[2][6];
      mainloop<true, 6>(acc, xf, wf, L, ask, look, f8a, f8b, st1, st2);
      STAMP(0, 2); STAMPC(0, 7);
      u4 hv;
      float et[8];
      auto epi = [&](int b, int ip, int st) {
        glu_stage(st, acc[2 * ip][b], acc[2 * ip + 1][b], acc[2 * ip][b + 3], acc[2 * ip + 1][b + 3], et, hv);
        if (st == 7) BS128(hv, rs, af + (6 * c + 3 * j + b) * 2048 + ip * 1024);
      };
#define NGR 15
#pragma unroll
      for (int pr = 0; pr < 4; ++pr)
#pragma unroll
        for (int k = 0; k < 16; ++k) {
          const int hf = k >> 3, b = pr + 3 * ((k >> 2) & 1), i = k & 3, r = (pr - 1) * 8 + (k >> 1);
          if (pr < 3) acc[i][b % 6] = mfma(wf[hf][b % 6], xf[hf][i], acc[i][b % 6]);
          SB0();
          if (pr == 0 && (k & 1)) b_dma(12 + (k >> 1));
          if (pr > 0) epi(pr - 1, hf, k & 7);
          if (pr > 0 && !(k & 1) && r < NGR) b_regs(r, r + 1);
          MEMB(); SB0();
        }
      STAMP(0, 3);
      b_regs(NGR, 15);
      asm volatile("s_waitcnt vmcnt(%0)" ::"i"(15 - NGR) : "memory");
      BARRIER();
      flag_store(rs, tid, FL_G + rt * 18 + c, tag);
      hv0 = ~tag;
    } else if (type == 2) {
      f4 acc[4][5];
      bf8 wf[2][5];
      mainloop<true, 5>(acc, xf, wf, L, ask, look, f8a, f8b, st1, st2);
      STAMP(0, 2); STAMPC(0, 7);
      u2 gr[4];
      const int row0 = rt * 256 + wm * 64, kidx = FL_KV + (lane & 7) * 8 + 2 * c + ((lane >> 3) & 1);
      u2 pq[4][6];
      float et[16], pt[16];
      u32 ot[8];
      gate_load(rs, row0, r16, gr);
#pragma unroll
      for (int b = 0; b < 6; ++b)
#pragma unroll
        for (int k = 0; k < 8; ++k) {
          if (b < 5) acc[k & 3][b % 5] = mfma(wf[k >> 2][b % 5], xf[k >> 2][k & 3], acc[k & 3][b % 5]);
          SB0();
          if (b == 0) b_dma(12 + k);
          else {
            phi_stage(k, acc[0][b - 1], acc[1][b - 1], acc[2][b - 1], acc[3][b - 1], et, pt, ot);
            if (k == 7) {
#pragma unroll
              for (int i = 0; i < 4; ++i) pq[i][b - 1] = u2{ot[2 * i], ot[2 * i + 1]};
#pragma unroll
              for (int ip = 0; ip < 2; ++ip)
                BS128((u4{ot[4 * ip], ot[4 * ip + 1], ot[4 * ip + 2], ot[4 * ip + 3]}), rs, af + (2 * KH + 10 * c + 5 * j + b - 1) * 2048 + ip * 1024);
            }
            const int r = (b - 1) * 4 + (k >> 1);
            if (!(k & 1) && r < 15) b_regs(r, r + 1);
          }
          MEMB(); SB0();
        }
#pragma unroll
      for (int i = 0; i < 4; ++i) pq[i][5] = u2{0u, 0u};
      gate_wait(rs, row0, r16, tag, gr);
      const int zq = lane - 10 * (lane >= 10);
      u32 zsrc = pick(lane < 20, (lane >= 10) * 384 + (zq >> 2) * 128 + (zq & 3) * 16, OOB), srcl[5];
#pragma unroll
      for (int o = 0; o < 5; ++o) { srcl[o] = pick((4 * o + g) / 5 == (r16 >> 2), 4 * o + g, 63) * 4; asm volatile("" : "+v"(srcl[o])); }
      asm volatile("" : "+v"(zsrc));
      STAMP(1, 7);
      if (w == 0) wait_flags(ws, kidx, tag, flag_load(ws, kidx));
      BARRIER();
      STAMP(1, 6);
      u4 zp[8];
#pragma unroll
      for (int r = 0; r < 8; ++r) zp[r] = BL128(rs, zsrc, ZP_OFF + (r * 16 + 4 * c + 2 * j) * 384);
      f4 z = __builtin_bit_cast(f4, zp[0]);
#pragma unroll
      for (int r = 1; r < 8; ++r) z += __builtin_bit_cast(f4, zp[r]);
      const u2 zb = pk4(z);
      f4 den[4];
#pragma unroll
      for (int i = 0; i < 4; ++i) den[i] = f4{0.f, 0.f, 0.f, 0.f};
#pragma unroll
      for (int s = 0; s < 3; ++s) {
        u4 zf;
#pragma unroll
        for (int o = 0; o < 2; ++o) {
          zf[2 * o] = 2 * s + o < 5 ? __builtin_amdgcn_ds_bpermute(srcl[(2 * s + o) % 5], zb[0]) : 0;
          zf[2 * o + 1] = 2 * s + o < 5 ? __builtin_amdgcn_ds_bpermute(srcl[(2 * s + o) % 5], zb[1]) : 0;
        }
#pragma unroll
        for (int i = 0; i < 4; ++i) den[i] = mfma(zf, u4{pq[i][2 * s][0], pq[i][2 * s][1], pq[i][2 * s + 1][0], pq[i][2 * s + 1][1]}, den[i]);
      }
      f4 sc;
#pragma unroll
      for (int i = 0; i < 4; ++i) sc[i] = __builtin_bit_cast(f2, gr[i])[0] * __builtin_amdgcn_rcpf(den[i][0] + 1e-6f);
      BS128(sc, rs, SC_OFF + ((rt * 4 + wm) * 32 + 8 * c + 4 * j) * 256 + vo);
      STAMP(0, 3);
      hv0 = flag_load(ws, hidx);
      VM0C(); VMCNT(0); BARRIER();
      flag_store(rs, tid, FL_S + rt * 4 + c, tag);
    } else {
      f4 acc[4][5];
      bf8 wf[2][5];
      mainloop<false, 5>(acc, xf, wf, L, ask, look, f8a, f8b, st1, st2);
      STAMP(0, 2); STAMPC(0, 7);
      constexpr int PA[8] = {0, 0, 1, 1, 1, 2, 2, 2}, PB[8] = {0, 1, 0, 1, 2, 1, 2, 0};
      u4 pk[2][3], vv[2][3];
      float et[16], pt[16];
      u32 ot[8];
#pragma unroll
      for (int b = 0; b < 6; ++b)
#pragma unroll
        for (int k = 0; k < 8; ++k) {
          if (b < 5) acc[k & 3][b % 5] = mfma(xf[k >> 2][k & 3], wf[k >> 2][b % 5], acc[k & 3][b % 5]);
          SB0();
          if (b > 0 && b < 4) {
            phi_stage(k, acc[0][b - 1], acc[1][b - 1], acc[2][b - 1], acc[3][b - 1], et, pt, ot);
            if (k == 7) { pk[0][b - 1] = u4{ot[0], ot[1], ot[2], ot[3]}; pk[1][b - 1] = u4{ot[4], ot[5], ot[6], ot[7]}; }
          }
          if (b >= 4) {
#pragma unroll
            for (int vb = (b == 4 ? 2 : 4); vb < (b == 4 ? 4 : 5); ++vb) {
              const u32 v = pk2(acc[k >> 1][vb][2 * (k & 1)], acc[k >> 1][vb][2 * (k & 1) + 1]);
              vv[k >> 2][vb - 2][k & 3] = vb == 2 ? pick(r16 == 0, 0x3f803f80u, v) : v;
            }
          }
          if (b < 5) SB0();
        }
#pragma unroll
      for (int n = 0; n < 8; ++n) {
        const f4 kv = mfma(vv[1][PB[n]], pk[1][PA[n]], mfma(vv[0][PB[n]], pk[0][PA[n]], f4{0.f, 0.f, 0.f, 0.f}));
        *(LAS f4*)(w * 8192 + (w >> 2) * 98304 + n * 1024 + lane * 16) = kv;
      }
      LGKM0(); BARRIER();
      const u32 zo = pick((w == 0 || w == 2 || w == 7) & (lane < 32), ((w + 1) / 3) * 128 + lane * 4, OOB);
#pragma unroll
      for (int j2 = 0; j2 < 2; ++j2) {
        f4 sum = *(LAS f4*)((j2 * 8 + w) * 1024 + lane * 16);
#pragma unroll
        for (int m = 1; m < 4; ++m) sum += *(LAS f4*)(((2 * m + j2) * 8 + w) * 1024 + (m >> 1) * 98304 + lane * 16);
        BS128(sum, rs, KVP_OFF + ((rt * 16 + 2 * c + j2) * 8 + w) * 1024 + vo);
        const float z0 = sum[0];
        __builtin_amdgcn_raw_buffer_store_b32(__builtin_bit_cast(u32, z0), rs, zo, ZP_OFF + (rt * 16 + 2 * c + j2) * 384, SC1);
      }
      STAMP(0, 3);
      VM0C(); VMCNT(0); LGKM0(); BARRIER();
      flag_store(rs, tid, FL_KV + rt * 8 + c, tag);
      STAMP(1, 7);
#pragma unroll
      for (int k = 12; k < 20; ++k) b_dma(k);
      b_regs(0, 15);
      hv0 = flag_load(ws, hidx);
      VM0C(); VMCNT(0);
    }
  }
  STAMP(0, 4);

  u32 pcv[3] = {w * 4096 + vo, w * 4096 + vo + 2 * XBUF, w * 4096 + vo + 4 * XBUF}, vp = vo;
  asm volatile("" : "+v"(pcv[0]), "+v"(pcv[1]), "+v"(pcv[2]), "+v"(vp));
  auto pc_addr = [&](int k) { return pcv[pc_base(k) >> 16] + (pc_base(k) & 0xffff); };
  const int ok0 = (o0 - KH) * 32, ok1 = ok0 + no * 32 - 1, mc = ok0 / 40 + (lane & 3), mc1 = ok1 / 40;
  int oidx = pick(lane < 4, FL_M + (mc < mc1 ? mc : mc1), FL_S + (rt2 >> 2) * 4 + pick(lane & 1, ok0, ok1) / 160);
  const int hb = ok0 / 20;
  u32 sco = 0;
#pragma unroll
  for (int e = 0; e < 6; ++e) sco |= (u32)((8 * (o0 - KH) + 4 * e + g) / 5 - hb) << (4 * e);
  asm volatile("" : "+v"(oidx), "+v"(sco));
  const u32 grow = GS_OFF + (rt2 * 64 + (w >> 1) * 16) * 8;
  u2 ge;
  {
#define OW 4
    constexpr int RA = 4;
    static_assert(OW == 4, "");
    f4 acc[4][5];
    u4 ar[RA][4], ao[2][4], bj[3], scl[2];
    u32 sa[10];
#pragma unroll
    for (int s = 0; s < 10; ++s) sa[s] = s < 7 ? pick(s == 6 && nh == 6, OOB, abase + (t0 + s) * 4096) : abase + (o0 + s - 7) * 4096;
    u4 ssrd = wssrd;
    ssrd[2] = SC_OFF + rt2 * 8192 + (ok1 / 20 + 1) * 256;
    auto lda1 = [&](int s, int i) { ar[s % RA][i] = BL128(rs, vp + i * 1024, sa[s]); };
    auto rds = [&](int s, int o) { scl[o] = *(LAS u4*)(pc_addr(16) - (vp & 0x300) + (((sco >> (4 * (2 * (s - 7) + o))) & 15) << 8)); };
    float slo, shi;
    auto scale1 = [&](int s, int q) {
      if (q > 0) {
        const int pi = (q - 1) >> 2, e = (q - 1) & 3;
        u32 r = pk2(slo, shi);
        asm volatile("" : "+v"(r));
        ao[s & 1][2 * (pi & 1) + (e >> 1)][2 * (pi >> 1) + (e & 1)] = r;
      }
      if (q < 16) {
        const int pi = q >> 2, e = q & 3;
        const float f = __builtin_bit_cast(f4, scl[pi >> 1])[2 * (pi & 1) + (e >> 1)];
        const u32 v = ar[s % RA][pi][e];
        slo = bflo(v) * f; shi = bfhi(v) * f;
        asm volatile("" : "+v"(slo), "+v"(shi));
      }
    };
    auto rdb = [&](int f) {
      if (f < 20) bj[f % 3] = *(LAS u4*)pc_addr(f);
      else if (f >= 35 && f < 50) {
        const int js = (f - 35) / 5, n = f % 5;
        const u32 a = n < 4 ? pc_addr(4 * js + (n >> 1)) + 8 * (n & 1) : pc_addr(12 + js) - (vp >> 1);
        const u2 lo = *(LAS u2*)a, hi = *(LAS u2*)(a + (n < 4 ? 2048 : 512));
        bj[f % 3] = u4{lo[0], lo[1], hi[0], hi[1]};
      }
    };
    auto oload = [&](int q) {
      if (q < 2) dma16n(q, pc_base(16) + w * 4096, vp, ssrd, SC_OFF + rt2 * 8192 + hb * 256);
      else {
        const int js = (q - 2) / 5, pp = (q - 2) % 5;
        dma16n(pp & 3, pc_base(pp < 4 ? 4 * js : 12 + js) + w * 4096, vp, wssrd, bbase + (o0 + js) * 5120 + (pp & 4) * 1024);
      }
    };
    rdb(0); rdb(1);
    wait_flags(ws, hidx, tag, hv0);
    STAMP(1, 0);
#pragma unroll
    for (int s = 0; s < RA - 1; ++s) {
#pragma unroll
      for (int i = 0; i < 4; ++i) lda1(s, i);
      SB0();
    }
    ge = BL64(rs, (vp & 0xf0) >> 1, grow);
    MEMB(); SB0();
#pragma unroll
    for (int q = 0; q < 4; ++q) unpair1(ar[0], q);
    asm volatile("s_nop 1");
    u32 fl = 0;
    auto step = [&](auto S, auto LAST) __attribute__((always_inline)) {
      constexpr int s = S.v;
      constexpr bool last = LAST.v;
      if (s == OW) { wait_flags(ws, oidx, tag, fl); STAMP(1, 2); }
      SB0();
#pragma unroll
      for (int k = 0; k < 20; ++k) {
        const int n = k >> 2, i = k & 3;
        const u4 bop = (s >= 4 && s < 7) ? breg[(s + 2) % 3][n] : bj[(5 * s + n) % 3];
        mma_in(acc[i][n], bop, s < 7 ? ar[s % RA][opix(i)] : ao[s & 1][i], s == 0);
        SB0();
        if (s == 6 && k == 0) { VM0C(); VMCNT(0); }
        if (s == 7 && k == 11)
          asm volatile("s_waitcnt vmcnt(4)\n\ts_cmp_eq_u32 %0, 3\n\ts_cbranch_scc1 1f\n\ts_waitcnt vmcnt(0)\n1:" ::"s"(no) : "memory", "scc");
        if (s == 8 && !last && k == 0) asm volatile("" : "+v"(ar[1][0]), "+v"(ar[1][1]), "+v"(ar[1][2]), "+v"(ar[1][3])::"memory");
        if (s == 8 && !last && k == 11) VMCNT(0);
        if (i == 0 && !(last && n >= 3)) rdb(5 * s + n + 2);
        if (!last && s >= 6 && k == 0) { rds(s + 1, 0); rds(s + 1, 1); }
        const int sn = s + RA - 1;
        if (s != 5 && k % 5 == 1 && sn < 9) lda1(sn, k / 5);
        if (k == 3 && s == OW - 1) fl = flag_load(ws, oidx);
        if (s == 4 && k % 5 == 3) oload(k / 5);
        if (s == 5 && k < 14 && !(k & 1)) { if (k & 2) oload(4 + k / 4); else lda1(8, k / 4); }
        if (s == 6 && k % 4 == 1) oload(7 + k / 4);
        if (s == 6 && k % 4 == 3 && k < 16)
          asm volatile("s_cmp_eq_u32 %4, 2\n\ts_cbranch_scc1 1f\n\tbuffer_load_dwordx4 %0, %1, %2, %3 offen offset:%5 sc1\n1:"
                       : "=&v"(ar[1][(k / 4) & 3]) : "v"(vp), "s"(wssrd), "s"(sa[9]), "s"(no), "i"(((k / 4) & 3) * 1024) : "memory", "scc");
        if (s == 7 && k % 3 == 1 && k < 15) {
          const int q = k / 3;
          asm volatile("s_cmp_eq_u32 %4, 2\n\ts_cbranch_scc1 1f\n\ts_mov_b32 m0, %0\n\ts_nop 0\n\tbuffer_load_dwordx4 %1, %2, %3 offen offset:%5 sc1 lds\n1:"
                       ::"s"(pc_base(q < 4 ? 8 : 14) + w * 4096), "v"(vp), "s"(wssrd), "s"(bbase + (o0 + 2) * 5120 + (q < 4 ? 0 : 4096)), "s"(no), "i"((q & 3) * 1024)
                       : "memory", "scc");
        }
        MEMB();
        if (!last) {
          if (s < 6 && k >= 12 && k < 16) unpair1(ar[(s + 1) % RA], k - 12);
          if (s >= 6 && k >= 1 && k < 18) scale1(s + 1, k - 1);
        } else if (n >= 1) *(LAS f4*)pc_addr(((0x31240 >> (4 * (n - 1))) & 15) * 4 + i) = acc[i][n - 1];
        SB0();
      }
      if (last || s == 7 || s == OW - 1) asm volatile("s_nop 7" ::: "memory");
      SB0();
      if (last) {
#pragma unroll
        for (int i = 0; i < 4; ++i) *(LAS f4*)pc_addr(12 + i) = acc[i][4];
      }
    };
    step(IC<0>{}, IC<0>{}); step(IC<1>{}, IC<0>{}); step(IC<2>{}, IC<0>{}); step(IC<3>{}, IC<0>{}); step(IC<4>{}, IC<0>{});
    step(IC<5>{}, IC<0>{}); step(IC<6>{}, IC<0>{}); step(IC<7>{}, IC<0>{});
    if (no == 2) step(IC<8>{}, IC<1>{});
    int no2 = no;
    asm volatile("" : "+s"(no2));
    if (no2 == 3) { step(IC<8>{}, IC<0>{}); step(IC<9>{}, IC<1>{}); }
    STAMP(1, 3);
  }
  for (u32 spin = 0; !__all(ge[1] == tag) && spin < SPIN_CAP; ++spin) {
    __builtin_amdgcn_s_sleep(2);
    MEMB();
    ge = BL64(rs, (vp & 0xf0) >> 1, grow);
  }
  const float om = __builtin_amdgcn_rcpf(1.f + __builtin_bit_cast(f2, ge)[0]);
  STAMP(1, 4);
  LGKM0(); BARRIER();
  {
    const int i = w >> 1, n0 = (w & 1) * 3;
    u16* op = out + (rt2 * 64 + i * 16 + ((vp >> 4) & 15)) * D_ + ct * 80 + n0 * 16 + ((vp >> 8) << 2);
#pragma unroll
    for (int n = 0; n < 3; ++n) {
      if (n0 + n < 5) {
        const u32 a = ((0x43201u >> (4 * (n0 + n))) & 15) * XBUF + i * 1024 + vp;
        f4 v = *(LAS f4*)a;
#pragma unroll
        for (int s = 1; s < 8; ++s) v += *(LAS f4*)(a + s * 4096);
        *(u2*)(op + n * 16) = pk4(v * om);
      }
    }
  }
  STAMP(1, 5);
}

extern "C" void kernel_launch(void* const* d_in, const int* in_sizes, int n_in, void* d_out, int out_size, void* d_ws, size_t ws_size,
                              hipStream_t stream) {
  (void)in_sizes; (void)n_in; (void)out_size; (void)ws_size;
  (void)hipFuncSetAttribute((const void*)fused, hipFuncAttributeMaxDynamicSharedMemorySize, LDS_BYTES);
  fused<<<256, 512, LDS_BYTES, stream>>>((const u16*)d_in[0], (const u16*)d_in[1], (const u16*)d_in[2], (const u16*)d_in[4], (const u16*)d_in[5],
                                         (const u16*)d_in[6], (const u16*)d_in[7], (char*)d_ws, (u16*)d_out);
}
```

```cpp
#include <hip/hip_runtime.h>
#include <stdint.h>

typedef unsigned short u16;
typedef unsigned int u32;
typedef unsigned long long u64;
typedef short s4 __attribute__((ext_vector_type(4)));
typedef __bf16 bf8 __attribute__((ext_vector_type(8)));
typedef __bf16 bf2 __attribute__((ext_vector_type(2)));
typedef float f2 __attribute__((ext_vector_type(2)));
typedef float f4 __attribute__((ext_vector_type(4)));
typedef u32 u4 __attribute__((ext_vector_type(4)));
typedef u32 u2 __attribute__((ext_vector_type(2)));
#define LAS __attribute__((address_space(3)))
#define GAS __attribute__((address_space(1)))
#define DEV __device__ __forceinline__
#define SB0() __builtin_amdgcn_sched_barrier(0)

constexpr int S_ = 2048, D_ = 640, F_ = 1728;
constexpr int KS2 = 74, KH = 54;
constexpr u32 AF_OFF = 0, AF_BYTES = 32u * KS2 * 4096u;
constexpr u32 BF_OFF = AF_OFF + AF_BYTES, BF_BYTES = 8u * KS2 * 5120u;
constexpr u32 KVP_OFF = BF_OFF + BF_BYTES, KVP_BYTES = 8u * 16u * 8192u;
constexpr u32 ZP_OFF = KVP_OFF + KVP_BYTES, ZP_BYTES = 8u * 16u * 384u;
constexpr u32 SC_OFF = ZP_OFF + ZP_BYTES, SC_BYTES = 32u * 32u * 256u;
constexpr u32 GS_OFF = SC_OFF + SC_BYTES, GS_BYTES = 2048u * 8u;
constexpr u32 FLAG_OFF = GS_OFF + GS_BYTES, FLAG_BYTES = 561u * 128u;
constexpr int FL_R = 0, FL_G = 256, FL_KV = 400, FL_Q = 464, FL_M = 496, FL_TA = 512, FL_S = 528, FL_EPOCH = 560;
constexpr u32 WS_BYTES = FLAG_OFF + FLAG_BYTES;
#define STAMP(k, i) do {} while (0)
#define STAMPC(k, i) do {} while (0)

constexpr u32 XBUF = 32768, WRING = 3 * XBUF, WSLOT = 12288, WPAD = 8192, LDS_BYTES = 163840;
constexpr u32 RPAD = WRING + 2 * WSLOT, SPARE = WRING + XBUF + 2 * WSLOT;
constexpr u32 OOB = 0x40000000u;
constexpr int SC1 = 16;
typedef __amdgpu_buffer_rsrc_t rsrc_t;
#define BL128(rs, vo, so) __builtin_amdgcn_raw_buffer_load_b128(rs, vo, so, SC1)
#define BL64(rs, vo, so) __builtin_amdgcn_raw_buffer_load_b64(rs, vo, so, SC1)
#define BS128(v, rs, off) __builtin_amdgcn_raw_buffer_store_b128(__builtin_bit_cast(u4, v), rs, off, 0, SC1)
#define RLX_AGENT __ATOMIC_RELAXED, __HIP_MEMORY_SCOPE_AGENT

#define RINGSC "sc1"
DEV u4 make_srd(const void* p, u32 bytes) {
  u64 a = (u64)p;
  u4 r = {(u32)a, (u32)(a >> 32) & 0xffffu, bytes, 0x00020000u};
  return r;
}
DEV void dma16(u32 lds, u32 voff, u4 srd, u32 soff) {
  asm volatile("s_mov_b32 m0, %0\n\ts_nop 0\n\tbuffer_load_dwordx4 %1, %2, %3 offen lds" ::"s"(lds), "v"(voff), "s"(srd), "s"(soff) : "memory");
}
template <int OFF> DEV void dma16o(u32 lds, u32 voff, u4 srd, u32 soff) {
  asm volatile("s_mov_b32 m0, %0\n\ts_nop 0\n\tbuffer_load_dwordx4 %1, %2, %3 offen offset:%4 " RINGSC " lds" ::"s"(lds), "v"(voff), "s"(srd), "s"(soff), "i"(OFF) : "memory");
}
DEV void dma16n(int d, u32 lds, u32 voff, u4 srd, u32 soff) {
  if (d == 0) dma16o<0>(lds, voff, srd, soff);
  else if (d == 1) dma16o<1024>(lds, voff, srd, soff);
  else if (d == 2) dma16o<2048>(lds, voff, srd, soff);
  else dma16o<3072>(lds, voff, srd, soff);
}
DEV void dma16c(u32 lds, u32 voff, u4 srd, u32 soff) {
  asm volatile("s_mov_b32 m0, %0\n\ts_nop 0\n\tbuffer_load_dwordx4 %1, %2, %3 offen sc1 lds" ::"s"(lds), "v"(voff), "s"(srd), "s"(soff) : "memory");
}
DEV void dma4c(u32 lds, u32 voff, u4 srd, u32 soff) {
  asm volatile("s_mov_b32 m0, %0\n\ts_nop 0\n\tbuffer_load_dword %1, %2, %3 offen sc1 lds" ::"s"(lds), "v"(voff), "s"(srd), "s"(soff) : "memory");
}
DEV s4 trread(u32 a) { return __builtin_amdgcn_ds_read_tr16_b64_v4i16((LAS s4*)a); }
DEV bf8 cat8(s4 lo, s4 hi) { return __builtin_bit_cast(bf8, __builtin_shufflevector(lo, hi, 0, 1, 2, 3, 4, 5, 6, 7)); }
DEV f4 mfma(bf8 a, bf8 b, f4 c) { return __builtin_amdgcn_mfma_f32_16x16x32_bf16(a, b, c, 0, 0, 0); }
DEV f4 mfma(u4 a, u4 b, f4 c) { return mfma(__builtin_bit_cast(bf8, a), __builtin_bit_cast(bf8, b), c); }
DEV u32 pk2(float a, float b) { f2 v = {a, b}; return __builtin_bit_cast(u32, __builtin_convertvector(v, bf2)); }
DEV u2 pk4(f4 v) { u2 r = {pk2(v[0], v[1]), pk2(v[2], v[3])}; return r; }
DEV u4 pk8(f4 a, f4 b) { const u2 x = pk4(a), y = pk4(b); return u4{x[0], x[1], y[0], y[1]}; }
DEV u32 pick(bool c, u32 a, u32 b) { return c ? a : b; }
DEV float bflo(u32 v) { return __builtin_bit_cast(float, v << 16); }
DEV float bfhi(u32 v) { return __builtin_bit_cast(float, v & 0xffff0000u); }
constexpr float LOG2E = 1.44269504089f;
DEV void phi_stage(int st, f4 t0, f4 t1, f4 t2, f4 t3, float (&e)[16], float (&p)[16], u32 (&o)[8]) {
  const f4 t[4] = {t0, t1, t2, t3};
#pragma unroll
  for (int q = 0; q < 16; ++q) {
    const float tq = t[q >> 2][q & 3];
    if (st == (q >> 3)) e[q] = tq * LOG2E;
    if (st == 1 + (q >> 2)) e[q] = __builtin_amdgcn_fmed3f(__builtin_amdgcn_exp2f(e[q]), 0.f, 1.f);
    if (st == (q < 4 ? 0 : 1 + (q >> 2))) p[q] = __builtin_amdgcn_fmed3f(tq, 1e-6f, __builtin_inff());
    if (st == 5 + (q >> 3)) { e[q] += p[q]; asm volatile("" : "+v"(e[q])); }
    if (st == 7 && (q & 1)) o[q >> 1] = pk2(e[q - 1], e[q]);
  }
}
DEV u4 glu_packed(f4 g0, f4 g1, f4 u0, f4 u1) {
  f2 e[4] = {f2{g0[0], g0[1]}, f2{g0[2], g0[3]}, f2{g1[0], g1[1]}, f2{g1[2], g1[3]}};
  const f2 u[4] = {f2{u0[0], u0[1]}, f2{u0[2], u0[3]}, f2{u1[0], u1[1]}, f2{u1[2], u1[3]}};
  u4 o;
#pragma unroll
  for (int q = 0; q < 4; ++q) e[q] *= -LOG2E;
#pragma unroll
  for (int q = 0; q < 4; ++q) e[q] = f2{__builtin_amdgcn_exp2f(e[q][0]), __builtin_amdgcn_exp2f(e[q][1])};
#pragma unroll
  for (int q = 0; q < 4; ++q) e[q] += 1.f;
#pragma unroll
  for (int q = 0; q < 4; ++q) e[q] = f2{__builtin_amdgcn_rcpf(e[q][0]), __builtin_amdgcn_rcpf(e[q][1])};
#pragma unroll
  for (int q = 0; q < 4; ++q) { e[q] *= u[q]; o[q] = pk2(e[q][0], e[q][1]); }
  return o;
}
DEV void glu_stage(int st, f4 g0, f4 g1, f4 u0, f4 u1, float (&e)[8], u4& o) {
  const f4 gg[2] = {g0, g1}, uu[2] = {u0, u1};
#pragma unroll
  for (int q = 0; q < 8; ++q) {
    if (st == 0) { e[q] = gg[q >> 2][q & 3] * -LOG2E; asm volatile("" : "+v"(e[q])); }
    if (st == 1 + (q >> 2)) e[q] = __builtin_amdgcn_exp2f(e[q]);
    if (st == 3) { e[q] += 1.f; asm volatile("" : "+v"(e[q])); }
    if (st == 4 + (q >> 2)) e[q] = __builtin_amdgcn_rcpf(e[q]);
    if (st == 6) { e[q] *= uu[q >> 2][q & 3]; asm volatile("" : "+v"(e[q])); }
    if (st == 7 && (q & 1)) o[q >> 1] = pk2(e[q - 1], e[q]);
  }
}
#define MEMB() asm volatile("" ::: "memory")
#define VM0C() __builtin_amdgcn_s_waitcnt(0x0F70)
#define SGB(mask, n) __builtin_amdgcn_sched_group_barrier(mask, n, 0)
#define LGKM0() __builtin_amdgcn_s_waitcnt(0xC07F)
#define VMCNT(n) asm volatile("s_waitcnt vmcnt(" #n ")" ::: "memory")
#define BARRIER() do { asm volatile("" ::: "memory"); __builtin_amdgcn_s_barrier(); asm volatile("" ::: "memory"); } while (0)

DEV u32 flag_load(char* ws, int idx) {
  asm volatile("" ::: "memory");
  return __builtin_amdgcn_raw_buffer_load_b32(__builtin_amdgcn_make_buffer_rsrc(ws, 0, WS_BYTES, 0x00020000), idx * 128, FLAG_OFF, SC1);
}
DEV void flag_store(__amdgpu_buffer_rsrc_t rs, u32 id, int idx, u32 v) {
  __builtin_amdgcn_raw_buffer_store_b32(v, rs, id < 32 ? id * 4 : OOB, FLAG_OFF + idx * 128, SC1);
  asm volatile("" ::: "memory");
}
constexpr u32 SPIN_CAP = 1u << 16;
DEV void wait_flags(char* ws, int idx, u32 tag, u32 v) {
#define PSLEEP 2
  for (u32 spin = 0; !__all(v == tag) && spin < SPIN_CAP; ++spin) {
    __builtin_amdgcn_s_sleep(PSLEEP);
    v = flag_load(ws, idx);
    asm volatile("" : "+v"(v));
  }
  asm volatile("" ::: "memory");
}
DEV u32 load_epoch(char* ws) {
  u32 e;
  asm volatile("s_load_dword %0, %1, 0x0\n\ts_waitcnt lgkmcnt(0)" : "=s"(e) : "s"(ws + FLAG_OFF + FL_EPOCH * 128) : "memory");
  return e;
}
DEV void gate_load(rsrc_t rs, int row0, int r16, u2 (&gr)[4]) {
#pragma unroll
  for (int i = 0; i < 4; ++i) gr[i] = BL64(rs, r16 * 8 + i * 128, GS_OFF + row0 * 8);
}
DEV void gate_wait(rsrc_t rs, int row0, int r16, u32 tag, u2 (&gr)[4]) {
  for (u32 spin = 0; !__all(gr[0][1] == tag && gr[1][1] == tag && gr[2][1] == tag && gr[3][1] == tag) && spin < SPIN_CAP; ++spin) {
    __builtin_amdgcn_s_sleep(2);
    asm volatile("" ::: "memory");
    gate_load(rs, row0, r16, gr);
    asm volatile("" : "+v"(gr[0]), "+v"(gr[1]), "+v"(gr[2]), "+v"(gr[3]));
  }
}

struct Loop {
  u4 xsrd, wsrd;
  u32 xso, wso, wpitch64;
  u32 xvo[4], wvo[3];
  u32 xdst, wdst;
  u32 xrd[2], wrd[5];
};
struct St { u32 xa, wa[5], rx, dw, dx, sw, sx; };
template <int NB> DEV u32 waddr(const u32 (&wa)[5], int b) { return NB == 6 ? wa[b % 3] + (b / 3) * 6144 : wa[b]; }

template <bool TR, int NB, int H, int MODE, class FN>
DEV void half_step(f4 (&acc)[4][NB], const bf8 (&xc)[4], const bf8 (&wc)[NB], bf8 (&xn)[4], bf8 (&wn)[NB], const Loop& L, St& s, FN fn) {
  constexpr int NA = NB == 6 ? 3 : 5;
  s4 lo[NB];
#pragma unroll
  for (int k = 0; k < 4 * NB; ++k) {
    const int b = k >> 2, i = k & 3;
    acc[i][b] = TR ? mfma(wc[b], xc[i], acc[i][b]) : mfma(xc[i], wc[b], acc[i][b]);
    SB0();
    if (MODE < 2) {
      if (k < 4) xn[k] = *(LAS bf8*)(s.xa + k * 2048);
      else if (k < 4 + 2 * NB) {
        const int nb = (k - 4) >> 1;
        const u32 a = waddr<NB>(s.wa, nb) + (H == 0 ? WSLOT : 0);
        if (k & 1) wn[nb] = cat8(lo[nb], trread(a + 768)); else lo[nb] = trread(a);
      }
      if (k == 4) {
        if (H == 0) {
          asm volatile("" : "+s"(s.rx));
          s.rx = s.rx == 2 * XBUF ? 0 : s.rx + XBUF;
        }
        s.xa = L.xrd[H] + s.rx;
        asm volatile("" : "+v"(s.xa));
      }
      if (H == 0 && k >= 4 + 2 * NB && k < 4 + 2 * NB + NA) {
        s.wa[k - 4 - 2 * NB] ^= WRING ^ (WRING + 2 * WSLOT + WPAD);
        asm volatile("" : "+v"(s.wa[k - 4 - 2 * NB]));
      }
    }
    if (MODE == 0) {
      if (k % NB == 1) {
        const int d = k / NB;
        if (H == 1) { if (d < 3) dma16n(d, s.dw, L.wvo[d], L.wsrd, s.sw); }
        else dma16n(d, s.dx, L.xvo[d], L.xsrd, s.sx);
      }
      if (k == 2) {
        if (H == 0) {
          asm volatile("" : "+s"(s.dw), "+s"(s.sw));
          s.dw ^= WRING ^ (WRING + 2 * WSLOT + WPAD); s.sw += L.wpitch64;
          asm volatile("" : "+s"(s.dw), "+s"(s.sw));
        } else {
          asm volatile("" : "+s"(s.dx), "+s"(s.sx));
          s.dx = s.dx >= 2 * XBUF ? s.dx - 2 * XBUF : s.dx + XBUF; s.sx += 128;
          asm volatile("" : "+s"(s.dx), "+s"(s.sx));
        }
      }
    }
    fn(k);
    SB0();
  }
}

template <bool TR, int NB, int N8A, class ASK, class LOOK, class F0, class F1, class ST, class ST2>
DEV void mainloop(f4 (&acc)[4][NB], bf8 (&xf)[2][4], bf8 (&wf)[2][NB], const Loop& L, ASK ask, LOOK look, F0 f8a, F1 f8b, ST st, ST2 st2) {
  constexpr int NA = NB == 6 ? 3 : 5;
  bf8 x1[4], w1[NB];
#pragma unroll
  for (int i = 0; i < 4; ++i)
#pragma unroll
    for (int b = 0; b < NB; ++b) { acc[i][b] = f4{0.f, 0.f, 0.f, 0.f}; asm volatile("" : "+v"(acc[i][b])); }
  St s;
  s.rx = 0; s.xa = L.xrd[1];
#pragma unroll
  for (int b = 0; b < NA; ++b) s.wa[b] = L.wrd[b];
  s.dw = L.wdst ^ (WRING ^ (WRING + 2 * WSLOT + WPAD)); s.sw = L.wso + L.wpitch64;
  s.dx = 2 * XBUF + L.xdst; s.sx = L.xso + 2 * 128;
  SB0();
  VMCNT(9);
  BARRIER();
  st();
#pragma unroll
  for (int i = 0; i < 4; ++i) xf[0][i] = *(LAS bf8*)(L.xrd[0] + i * 2048);
#pragma unroll
  for (int b = 0; b < NB; ++b) { const u32 a = waddr<NB>(L.wrd, b); wf[0][b] = cat8(trread(a), trread(a + 768)); }
  auto nop = [](int) {};
#pragma unroll 1
  for (int t = 0; t < 8; ++t) {
    SB0(); LGKM0(); SB0();
    half_step<TR, NB, 0, 0>(acc, xf[0], wf[0], x1, w1, L, s, nop);
    SB0(); VMCNT(4); LGKM0(); BARRIER(); SB0();
    half_step<TR, NB, 1, 0>(acc, x1, w1, xf[0], wf[0], L, s, [&](int k) { ask(t, k); });
  }
  look();
  SB0(); LGKM0(); SB0();
  half_step<TR, NB, 0, 1>(acc, xf[0], wf[0], x1, w1, L, s, f8a);
  SB0();
  asm volatile("s_waitcnt vmcnt(%0)" ::"i"(N8A) : "memory");
  LGKM0(); BARRIER(); SB0();
  s4 lo[NB];
  half_step<TR, NB, 1, 1>(acc, x1, w1, xf[0], wf[0], L, s, [&](int k) {
    const int r = k - 2;
    if (r >= 0 && r < 2 * NB) {
      const u32 a = waddr<NB>(s.wa, r >> 1) + WSLOT;
      if (r & 1) wf[1][r >> 1] = cat8(lo[r >> 1], trread(a + 768)); else lo[r >> 1] = trread(a);
    } else if (r >= 2 * NB && r < 2 * NB + 4) xf[1][r - 2 * NB] = *(LAS bf8*)(L.xrd[1] + (r - 2 * NB) * 2048);
    f8b(k);
  });
  SB0(); LGKM0(); BARRIER(); SB0();
}

DEV constexpr u32 pc_base(int k) { return (k < 4 ? XBUF : k < 8 ? WRING : k < 12 ? 2 * XBUF : k < 16 ? WRING + XBUF : 0) + (k & 3) * 1024; }

DEV void unpair1(u4 (&p)[4], int q) {
  const int ip = q >> 1, e = q & 1;
  u32 x = p[ip][2 + e], y = p[2 + ip][e];
  asm volatile("v_swap_b32 %0, %1" : "+v"(x), "+v"(y));
  p[ip][2 + e] = x; p[2 + ip][e] = y;
}
DEV void mma_in(f4& c, u4 a, u4 b, bool first) {
  if (first) asm volatile("v_mfma_f32_16x16x32_bf16 %0, %1, %2, 0" : "=&v"(c) : "v"(a), "v"(b));
  else asm volatile("v_mfma_f32_16x16x32_bf16 %0, %1, %2, %0" : "+v"(c) : "v"(a), "v"(b));
}
DEV constexpr int opix(int i) { return 2 * (i & 1) + (i >> 1); }
template <int V> struct IC { static constexpr int v = V; };

__global__ __launch_bounds__(512, 2) void fused(const u16* __restrict__ x, const u16* __restrict__ wqkv, const u16* __restrict__ wo,
                                                const u16* __restrict__ wg, const u16* __restrict__ wu, const u16* __restrict__ wd,
                                                const u16* __restrict__ gw, char* ws, u16* __restrict__ out) {
  extern __shared__ __attribute__((aligned(16))) char smem[];
  int lane = threadIdx.x & 63, w = __builtin_amdgcn_readfirstlane(threadIdx.x >> 6), bid = blockIdx.x;
  asm volatile("" ::"s"(x), "s"(wqkv), "s"(wo), "s"(wg), "s"(wu), "s"(wd), "s"(gw), "s"(ws), "s"(out));
  Loop L;
  int rt = 0, cq = 0, type = 0, c = 0;
  STAMP(0, 0);
  if (bid < 240) {
    const int xcd = bid & 7, jj = bid >> 3;
    const int idx = (xcd >> 1) * 30 + jj;
    rt = (xcd & 1) * 4 + (idx & 3); cq = idx >> 2;
    L.xsrd = make_srd(x, S_ * D_ * 2);
    L.xso = (rt * 256 + w * 32) * 1280;
#pragma unroll
    for (int i = 0; i < 4; ++i) L.xvo[i] = (lane >> 3) * 1280 + (((lane & 7) ^ ((4 * i + (lane >> 4)) & 7)) << 4) + i * (10240 - 1024);
    L.xdst = w * 4096;
    asm volatile("s_nop 4");
#pragma unroll
    for (int i = 0; i < 4; ++i) dma16n(i, L.xdst, L.xvo[i], L.xsrd, L.xso);
    asm volatile("" : "+s"(cq), "+s"(w)); asm volatile("" : "+v"(lane));
    __builtin_assume(w >= 0 && w < 8 && lane >= 0 && lane < 64 && cq >= 0 && cq < 30);
    const int c15 = cq >= 15 ? cq - 15 : cq, up = cq >= 15;
    const int r8 = c15 & 7, h8 = c15 >> 3;
    type = (0x02100100 >> (4 * r8)) & 15;
    const int ord = (0x40132010 >> (4 * r8)) & 15;
    c = up * pick(type == 0, 9, 4 >> ((type - 1) & 1)) + h8 * pick(type == 0, 5, 2 >> ((type - 1) & 1)) + ord;
    {
      const int part = w >> 2, wq = w & 3;
      const u32 pitch = type == 0 ? F_ * 2 : 3 * D_ * 2;
      const u32 colbase = pick(type == 0, 96 * c, pick(type == 1, 640 + 640 * part + 80 * c, 160 * c + 80 * part));
      const int nch = type == 0 ? 12 : 10;
      L.wsrd = make_srd(type == 0 ? (part ? wu : wg) : wqkv, D_ * pitch);
      L.wpitch64 = 64 * pitch;
      L.wso = 16 * wq * pitch + colbase * 2;
#pragma unroll
      for (int i = 0; i < 3; ++i) {
        const int n = i * 64 + lane, row = n / 12, ch = (n % 12) ^ (((row >> 3) & 1) << 1);
        L.wvo[i] = pick(ch < nch, row * pitch + (ch << 4) - i * 1024, OOB);
      }
      L.wdst = WRING + (wq >> 1) * WSLOT + part * 6144 + (wq & 1) * 3072;
    }
    asm volatile("s_nop 4");
#pragma unroll
    for (int i = 0; i < 3; ++i) dma16n(i, L.wdst, L.wvo[i], L.wsrd, L.wso);
    BARRIER();
#pragma unroll
    for (int i = 0; i < 4; ++i) dma16n(i, XBUF + L.xdst, L.xvo[i], L.xsrd, L.xso + 128);
#pragma unroll
    for (int i = 0; i < 3; ++i) dma16n(i, L.wdst + XBUF, L.wvo[i], L.wsrd, L.wso + L.wpitch64);
  }
  asm volatile("" : "+s"(bid), "+s"(w)); asm volatile("" : "+v"(lane));
  __builtin_assume(w >= 0 && w < 8 && lane >= 0 && lane < 64 && bid >= 0 && bid < 256);
  const int tid = w * 64 + lane, xcd = bid & 7, jj = bid >> 3;
  const int g = lane >> 4, r16 = lane & 15;
  const rsrc_t rs = __builtin_amdgcn_make_buffer_rsrc(ws, 0, WS_BYTES, 0x00020000);
  const u4 wssrd = make_srd(ws, WS_BYTES);
  const u32 vo = lane * 16;
#define MAP2 1
  const int rt2 = MAP2 == 0 ? (xcd >> 1) * 8 + (jj >> 2) : xcd * 4 + (jj >> 3), ct = MAP2 == 0 ? (xcd & 1) * 4 + (jj & 3) : (jj & 7);
  const u32 abase = AF_OFF + rt2 * (KS2 * 4096u), bbase = BF_OFF + ct * (KS2 * 5120u);
  const int t0 = 7 * w - (w == 7), nh = 7 - (w >= 6), o0 = KH + pick(w < 4, 3 * w, 2 * w + 4), no = 2 + (w < 4);
  u4 breg[3][5];
  auto b_dma = [&](int k) { dma16n(k & 3, pc_base(k & ~3) + w * 4096, vo, wssrd, bbase + t0 * 5120 + (k & ~3) * 1024); };
  auto b_regs = [&](int k0, int k1) {
    const u32 sb = bbase + (t0 + 4) * 5120;
#pragma unroll
    for (int k = k0; k < k1; ++k) breg[k / 5][k % 5] = BL128(rs, vo + (k & 3) * 1024, pick(k >= 10 && nh == 6, OOB, sb + (k >> 2) * 4096));
  };
#define NBR 15
  auto b_reg_if = [&](int k, bool again) {
    const u32 so = pick(k >= 10 && nh == 6, OOB, bbase + (t0 + 4) * 5120 + (k >> 2) * 4096);
    if (!again)
      asm volatile("s_cmp_lg_u32 %4, 0\n\ts_cbranch_scc1 1f\n\tbuffer_load_dwordx4 %0, %1, %2, %3 offen offset:%5 sc1\n1:"
                   : "=&v"(breg[k / 5][k % 5]) : "v"(vo), "s"(wssrd), "s"(so), "s"(w), "i"((k & 3) * 1024) : "memory", "scc");
    else
      asm volatile("s_cmp_eq_u32 %4, 0\n\ts_cbranch_scc1 1f\n\tbuffer_load_dwordx4 %0, %1, %2, %3 offen offset:%5 sc1\n1:"
                   : "+v"(breg[k / 5][k % 5]) : "v"(vo), "s"(wssrd), "s"(so), "s"(w), "i"((k & 3) * 1024) : "memory", "scc");
  };
  const u4 wdsrd = make_srd(wd, F_ * D_ * 2);
  auto r_in = [&](int u, u32 lds) {
    const int row = 4 * w + g;
    dma16(lds + w * 1024, row * 1280 + ((r16 ^ ((row & 7) << 1)) << 4), wdsrd, (u / 5) * 40960 + (u % 5) * 256);
  };
  auto r_out = [&](int u, u32 lds) {
    const int row = 4 * g + (r16 >> 2), n40 = (u % 5) * 8 + w;
    const u32 a = lds + row * 256 + (((2 * w + ((lane >> 1) & 1)) ^ ((row & 7) << 1)) << 4) + 8 * (lane & 1);
    BS128(cat8(trread(a), trread(a + 4096)), rs, vo + BF_OFF + (((n40 / 5) * KS2 + u / 5) * 5 + n40 % 5) * 1024);
  };
  const int hidx = pick(w == 0, FL_G + (rt2 >> 2) * 18 + (lane < 18 ? lane : 17), OOB / 128);
  const int ok0 = (o0 - KH) * 32, ok1 = ok0 + no * 32 - 1, hb = ok0 / 20;
  int oidx;
  u32 sco;
  auto setup2 = [&]() {
    u32 l = vo >> 4;
    asm volatile("" : "+v"(l));
    const int mc = ok0 / 40 + (l & 3), mc1 = ok1 / 40;
    oidx = pick(l < 4, FL_M + (mc < mc1 ? mc : mc1), FL_S + (rt2 >> 2) * 4 + pick(l & 1, ok0, ok1) / 160);
    sco = 0;
#pragma unroll
    for (int e = 0; e < 6; ++e) sco |= (u32)((8 * (o0 - KH) + 4 * e + (l >> 4)) / 5 - hb) << (4 * e);
    asm volatile("" : "+v"(oidx), "+v"(sco));
  };
  u32 tag, hv0;
  if (jj >= 30) {
    const int tw = (jj - 30) * 8 + xcd;
    u32 so[5];
#pragma unroll
    for (int i = 0; i < 5; ++i) { const int n = i * 64 + lane, row = 4 * w + n / 80, p = n % 80; so[i] = row * 1280 + ((p ^ ((row & 7) << 1)) << 4); }
    const u4 wosrd = make_srd(wo, D_ * D_ * 2);
    asm volatile("s_nop 4");
#pragma unroll
    for (int i = 0; i < 8; ++i) r_in(144 + 16 * i + tw, 2 * XBUF + i * 8192);
    if (w < 5) {
#pragma unroll
      for (int s = 0; s < 2; ++s)
#pragma unroll
        for (int i = 0; i < 5; ++i) dma16(s * 25600 + w * 5120 + i * 1024, so[i], wosrd, (tw * 2 + s) * 25600);
    }
    tag = load_epoch(ws) + 1u;
    const int q = r16 >> 2, p = lane & 3;
    VMCNT(0); BARRIER();
#pragma unroll
    for (int i = 0; i < 8; ++i)
      if (i < 7 || tw < 14) r_out(144 + 16 * i + tw, 2 * XBUF + i * 8192);
    VM0C(); VMCNT(0); LGKM0(); BARRIER();
    flag_store(rs, tid, FL_R + bid, tag);
    STAMP(0, 1);
    u4 xv[16], xt[4], gv[2];
    {
      const u4* xp = (const u4*)(x + (tw * 128 + w * 16) * D_);
      gv[0] = ((const u4*)gw)[lane]; gv[1] = ((const u4*)gw)[64 + r16];
#pragma unroll
      for (int r = 0; r < 16; ++r) xv[r] = xp[r * 80 + lane];
#pragma unroll
      for (int i = 0; i < 4; ++i) xt[i] = xp[(4 * i + g) * 80 + 64 + r16];
    }
    {
      auto dot8 = [](u4 a, u4 b) {
        float s0 = 0.f;
#pragma unroll
        for (int e = 0; e < 4; ++e) { const u32 ae = a[e], be = b[e]; s0 = __builtin_amdgcn_fdot2_f32_bf16(__builtin_bit_cast(bf2, ae), __builtin_bit_cast(bf2, be), s0, false); }
        return s0;
      };
      float v[16];
#pragma unroll
      for (int r = 0; r < 16; ++r) v[r] = dot8(xv[r], gv[0]);
#pragma unroll
      for (int i = 0; i < 4; ++i) {
        const float tl = dot8(xt[i], gv[1]);
#pragma unroll
        for (int e = 0; e < 4; ++e) v[4 * i + e] += g == e ? tl : 0.f;
      }
#pragma unroll
      for (int b = 0; b < 4; ++b) {
        const bool hi = (lane >> b) & 1;
#pragma unroll
        for (int r = 0; r < (8 >> b); ++r) {
          const float keep = hi ? v[2 * r + 1] : v[2 * r], send = hi ? v[2 * r] : v[2 * r + 1];
          v[r] = keep + __shfl_xor(send, 1 << b);
        }
      }
      float a0 = v[0];
      a0 += __shfl_xor(a0, 16); a0 += __shfl_xor(a0, 32);
      const float gate = __builtin_amdgcn_exp2f(LOG2E * __builtin_amdgcn_fmed3f(a0, -60.f, 60.f));
      __builtin_amdgcn_raw_buffer_store_b64(u2{__builtin_bit_cast(u32, gate), tag}, rs, pick(lane < 16, lane * 8, OOB), GS_OFF + (tw * 128 + w * 16) * 8, SC1);
    }
    STAMP(0, 5);
    if (w == 0) {
      u32 v[4];
#pragma unroll
      for (int k = 0; k < 4; ++k) v[k] = flag_load(ws, FL_R + k * 64 + lane);
#pragma unroll
      for (int k = 0; k < 4; ++k) wait_flags(ws, FL_R + k * 64 + lane, tag, v[k]);
      flag_store(rs, lane, FL_TA + tw, tag);
      flag_store(rs, lane, pick(tw == 0, FL_EPOCH, OOB / 128), tag);
      STAMP(1, 1);
    }
    bf8 wt[2][5];
#pragma unroll
    for (int s = 0; s < 2; ++s)
#pragma unroll
      for (int nb = 0; nb < 5; ++nb) {
        const int ch = 2 * (w * 5 + nb) + (p >> 1);
        s4 v[2];
#pragma unroll
        for (int t = 0; t < 2; ++t) {
          const int e0 = 8 * g + 4 * t, row = (e0 < 20 ? e0 : 0) + q;
          v[t] = trread(s * 25600 + row * 1280 + ((ch ^ ((row & 7) << 1)) << 4) + 8 * (p & 1));
          if (e0 >= 20) v[t] = s4{0, 0, 0, 0};
        }
        wt[s][nb] = cat8(v[0], v[1]);
      }
    LGKM0(); BARRIER();
    *(LAS u2*)(tid * 8) = u2{0u, 0u};
    LGKM0(); BARRIER();
#pragma unroll
    for (int k = 0; k < 16; ++k) b_dma(k);
    if (w >= 2) { b_dma(16); b_dma(17); b_dma(18); b_dma(19); }
    b_regs(0, 15);
    const int dj = 16 * ((0x22211100 >> (4 * w)) & 15) + r16, ej = 16 * ((0x02121010 >> (4 * w)) & 15) + 4 * g - 8, hh = dj >= 20;
    u32 kdst = pick((dj < 40) & (ej >= 0) & ((ej >= 20) == hh), (dj + 16 * hh) * 64 + (ej - 20 * hh) * 2, 4096 + lane * 8), mdst[3], md16[3];
#pragma unroll
    for (int sb = 0; sb < 3; ++sb) {
      const int q4 = 10 * tw + 4 * sb + g;
      const u32 st = pick(sb < 2 || g < 2, BF_OFF + (w * KS2 + KH + (q4 >> 3)) * 5120u, OOB), hf = (q4 >> 2) & 1, ln = (q4 & 3) * 16 + r16;
      mdst[sb] = st + 4096 + hf * 512 + ln * 8; md16[sb] = st + hf * 2048 + ln * 16;
      asm volatile("" : "+v"(mdst[sb]), "+v"(md16[sb]));
    }
    asm volatile("" : "+v"(kdst));
    setup2();
    VM0C();
    if (w == 0) wait_flags(ws, FL_KV + (lane & 7) * 8 + (tw >> 1), tag, flag_load(ws, FL_KV + (lane & 7) * 8 + (tw >> 1)));
    BARRIER();
    STAMP(1, 6);
    {
      u4 pv[8];
#pragma unroll
      for (int r = 0; r < 8; ++r) pv[r] = BL128(rs, vo, pick(w < 7, KVP_OFF + ((r * 16 + tw) * 8 + w) * 1024, OOB));
      f4 sum = __builtin_bit_cast(f4, pv[0]);
#pragma unroll
      for (int r = 1; r < 8; ++r) sum += __builtin_bit_cast(f4, pv[r]);
      *(LAS u2*)kdst = pk4(sum);
    }
    LGKM0(); BARRIER();
    STAMP(0, 2);
    bf8 ka[4];
#pragma unroll
    for (int sb = 0; sb < 4; ++sb) ka[sb] = *(LAS bf8*)((sb * 16 + r16) * 64 + g * 16);
#pragma unroll
    for (int sb = 0; sb < 3; ++sb) {
      f4 m[5];
#pragma unroll
      for (int nb = 0; nb < 5; ++nb) {
        m[nb] = mfma(ka[sb == 2 ? 3 : sb], wt[sb >> 1][nb], f4{0.f, 0.f, 0.f, 0.f});
        if (sb == 1) m[nb] = mfma(ka[2], wt[1][nb], m[nb]);
      }
      BS128(pk8(m[0], m[1]), rs, md16[sb]);
      BS128(pk8(m[2], m[3]), rs, md16[sb] + 1024);
      __builtin_amdgcn_raw_buffer_store_b64(pk4(m[4]), rs, mdst[sb], 0, SC1);
    }
    STAMP(0, 3);
    hv0 = flag_load(ws, hidx);
    VM0C(); VMCNT(0); BARRIER();
    flag_store(rs, tid, FL_M + tw, tag);
    STAMP(1, 7);
    if (w < 2) { b_dma(16); b_dma(17); b_dma(18); b_dma(19); }
  } else {
    const int wm = w >> 1, j = w & 1;
    const int nk = pick(type == 0, rt * 18 + c, 1 << 16);
    r_in(nk, RPAD);
    SB0();
    {
#pragma unroll
      for (int s = 0; s < 2; ++s) L.xrd[s] = (64 * wm + r16) * 128 + (((4 * s + g) ^ (r16 >> 1)) << 4);
      const int q = r16 >> 2, p = lane & 3;
#pragma unroll
      for (int b = 0; b < 5; ++b) {
        const int u = 2 * b + (p >> 1);
        const int ch = pick(type == 0, 6 * j + u, pick(type == 1, 5 * j + u - 5 * (u >= 5), u));
        const int part = pick(type == 0, 0, pick(type == 1, u >= 5, j));
        L.wrd[b] = WRING + part * 6144 + (8 * g + q) * 192 + ((ch ^ ((g & 1) << 1)) << 4) + 8 * (p & 1);
      }
    }
#define NB1 4
#define TB2 2
    const int wq = w & 3;
    const bool early = w < 4;
    u32 tch, tso;
    u4 tsrd;
    {
      const int u = nk + 144 * wq, st = (early ? 2 : 6) + u / 84, r = u % 84;
      const u64 p0 = (u64)wqkv, p1 = (u64)wg, p2 = (u64)wu, p12 = r < 57 ? +p1 : +p2, pw = r < 30 ? +p0 : +p12, pp = wq < 3 ? +pw : (u64)x;
      tsrd = make_srd((const void*)pp, pick(wq == 3, S_ * D_ * 2, pick(u >= 336, 0, pick(r < 30, 3 * D_ * D_ * 2, F_ * D_ * 2))));
      const int li = nk * 57 + lane;
      tch = wq < 3 ? lane * 128 : pick((lane < 57) & (li < 8192), (li >> 2) * 1280 + ((early ? 2 : 6) + (li & 3)) * 128, OOB);
      tso = pick(wq < 3, pick(r < 30, st * 30 + r, st * 27 + r - pick(r < 57, 30, 57)) * 8192, 0);
      asm volatile("" : "+v"(tch));
    }
    const u32 twhen = pick(early || type != 0, 99, TB2);
    dma4c(SPARE + 2048 + w * 256, tch, tsrd, pick(early, tso, OOB));
    tag = load_epoch(ws) + 1u;
    auto st1 = [&]() { STAMP(0, 1); STAMPC(0, 6); };
    auto st2 = [&](int t) {
    };
    const u32 af = AF_OFF + (u32)(rt * 4 + wm) * (KS2 * 4096u) + vo;
    const u32 tadr = FLAG_OFF + (FL_TA + (jj & 1) * 8 + xcd) * 128;
    u32 rlds;
    {
      const int row = 4 * g + (r16 >> 2);
      rlds = RPAD + row * 256 + (((2 * w + ((lane >> 1) & 1)) ^ ((row & 7) << 1)) << 4) + 8 * (lane & 1);
      asm volatile("" : "+v"(rlds));
    }
    const int n40 = (nk % 5) * 8 + w;
    const u32 rdst = BF_OFF + (((n40 / 5) * KS2 + nk / 5) * 5 + n40 % 5) * 1024, hooks = pick(type != 0, 0x42, 0x43);
    u4 fsrd = wssrd;
    fsrd[2] = pick(w == 0, FLAG_OFF + (FL_R + bid + 1) * 128, 0u);
    auto ask = [&](int t, int k) {
      if (k == 0) {
        asm volatile("s_cmp_lg_u32 %0, %1\n\ts_cbranch_scc1 1f\n\ts_mov_b32 m0, %2\n\ts_nop 0\n\tbuffer_load_dword %3, %4, %5 offen sc1 lds\n1:"
                     ::"s"(t), "s"(twhen), "s"(SPARE + 2048 + w * 256), "v"(tch), "s"(tsrd), "s"(tso) : "memory", "scc");
        asm volatile(
            "s_bitcmp1_b32 %10, %0\n\t"
            "s_cbranch_scc0 3f\n\t"
            "s_cmp_eq_u32 %0, 1\n\t"
            "s_cbranch_scc1 1f\n\t"
            "s_cmp_eq_u32 %0, 6\n\t"
            "s_cbranch_scc1 2f\n\t"
            "ds_read_b64_tr_b16 v[252:253], %1\n\t"
            "ds_read_b64_tr_b16 v[254:255], %1 offset:4096\n\t"
            "s_waitcnt lgkmcnt(0)\n\t"
            "buffer_store_dwordx4 v[252:255], %2, %3, %4 offen sc1\n\t"
            "s_nop 1\n\t"
            "s_branch 3f\n"
            "1:\n\t"
            "v_mov_b32 v252, %7\n\t"
            "v_lshrrev_b32 v253, 2, %2\n\t"
            "buffer_store_dword v252, v253, %5, %6 offen sc1\n\t"
            "s_branch 3f\n"
            "2:\n\t"
            "s_mov_b32 m0, %8\n\t"
            "s_nop 0\n\t"
            "buffer_load_dword off, %3, %9 sc1 lds\n"
            "3:"
            :
            : "s"(t), "v"(rlds), "v"(vo), "s"(wssrd), "s"(rdst), "s"(fsrd), "s"(FLAG_OFF + (FL_R + bid) * 128), "s"(tag), "s"(SPARE + w * 256), "s"(tadr), "s"(hooks)
            : "memory", "scc", "v252", "v253", "v254", "v255");
      }
    };
    auto look = [&]() {
      u32 v = *(LAS u32*)(SPARE + w * 256);
      for (u32 spin = 0; __builtin_amdgcn_readfirstlane(v) != tag && spin < SPIN_CAP; ++spin) {
        __builtin_amdgcn_s_sleep(2);
        asm volatile("buffer_load_dword %0, off, %1, %2 sc1\n\ts_waitcnt vmcnt(0)" : "=v"(v) : "s"(wssrd), "s"(tadr) : "memory");
      }
    };
    auto f8a = [&](int k) { if (k % 5 == 1 && k < 20) b_dma(k / 5); };
    auto f8b = [&](int k) { if (k % 2 == 1 && k < 16) b_dma(4 + k / 2); };
    bf8 xf[2][4];
    if (type == 0) {
      f4 acc[4][6];
      bf8 wf[2][6];
      mainloop<true, 6, 4>(acc, xf, wf, L, ask, look, f8a, f8b, st1, st2);
      STAMP(0, 2); STAMPC(0, 7);
      u4 hv;
      float et[8];
      auto epi = [&](int b, int ip, int st) {
        glu_stage(st, acc[2 * ip][b], acc[2 * ip + 1][b], acc[2 * ip][b + 3], acc[2 * ip + 1][b + 3], et, hv);
        if (st == 7) BS128(hv, rs, af + (6 * c + 3 * j + b) * 2048 + ip * 1024);
      };
#define NGR 15
#pragma unroll
      for (int pr = 0; pr < 4; ++pr)
#pragma unroll
        for (int k = 0; k < 16; ++k) {
          const int hf = k >> 3, b = pr + 3 * ((k >> 2) & 1), i = k & 3, r = (pr - 1) * 8 + (k >> 1);
          if (pr < 3) acc[i][b % 6] = mfma(wf[hf][b % 6], xf[hf][i], acc[i][b % 6]);
          SB0();
          if (pr == 0 && (k & 1)) b_dma(12 + (k >> 1));
          if (pr > 0) epi(pr - 1, hf, k & 7);
          if (pr > 0 && !(k & 1) && r < 15) b_reg_if(r, false);
          MEMB(); SB0();
        }
      STAMP(0, 3);
#define NPRE 6
      VM0C();
      VMCNT(0);
#pragma unroll
      for (int k = 0; k < NPRE; ++k) b_reg_if(k, true);
      BARRIER();
      flag_store(rs, tid, FL_G + rt * 18 + c, tag);
#pragma unroll
      for (int k = NPRE; k < 15; ++k) b_reg_if(k, true);
      setup2();
      hv0 = ~tag;
    } else if (type == 2) {
      f4 acc[4][5];
      bf8 wf[2][5];
      mainloop<true, 5, 4>(acc, xf, wf, L, ask, look, f8a, f8b, st1, st2);
      STAMP(0, 2); STAMPC(0, 7);
      u2 gr[4];
      const int row0 = rt * 256 + wm * 64, kidx = FL_KV + (lane & 7) * 8 + 2 * c + ((lane >> 3) & 1);
      u2 pq[4][6];
      float et[16], pt[16];
      u32 ot[8];
      gate_load(rs, row0, r16, gr);
#pragma unroll
      for (int b = 0; b < 6; ++b)
#pragma unroll
        for (int k = 0; k < 8; ++k) {
          if (b < 5) acc[k & 3][b % 5] = mfma(wf[k >> 2][b % 5], xf[k >> 2][k & 3], acc[k & 3][b % 5]);
          SB0();
          if (b == 0) b_dma(12 + k);
          else {
            phi_stage(k, acc[0][b - 1], acc[1][b - 1], acc[2][b - 1], acc[3][b - 1], et, pt, ot);
            if (k == 7) {
#pragma unroll
              for (int i = 0; i < 4; ++i) pq[i][b - 1] = u2{ot[2 * i], ot[2 * i + 1]};
#pragma unroll
              for (int ip = 0; ip < 2; ++ip)
                BS128((u4{ot[4 * ip], ot[4 * ip + 1], ot[4 * ip + 2], ot[4 * ip + 3]}), rs, af + (2 * KH + 10 * c + 5 * j + b - 1) * 2048 + ip * 1024);
            }
            const int r = (b - 1) * 4 + (k >> 1);
            if (!(k & 1) && r < 15) b_regs(r, r + 1);
          }
          MEMB(); SB0();
        }
#pragma unroll
      for (int i = 0; i < 4; ++i) pq[i][5] = u2{0u, 0u};
      gate_wait(rs, row0, r16, tag, gr);
      const int zq = lane - 10 * (lane >= 10);
      u32 zsrc = pick(lane < 20, (lane >= 10) * 384 + (zq >> 2) * 128 + (zq & 3) * 16, OOB), srcl[5];
#pragma unroll
      for (int o = 0; o < 5; ++o) { srcl[o] = pick((4 * o + g) / 5 == (r16 >> 2), 4 * o + g, 63) * 4; asm volatile("" : "+v"(srcl[o])); }
      asm volatile("" : "+v"(zsrc));
      setup2();
      STAMP(1, 7);
      if (w == 0) wait_flags(ws, kidx, tag, flag_load(ws, kidx));
      BARRIER();
      STAMP(1, 6);
      u4 zp[8];
#pragma unroll
      for (int r = 0; r < 8; ++r) zp[r] = BL128(rs, zsrc, ZP_OFF + (r * 16 + 4 * c + 2 * j) * 384);
      f4 z = __builtin_bit_cast(f4, zp[0]);
#pragma unroll
      for (int r = 1; r < 8; ++r) z += __builtin_bit_cast(f4, zp[r]);
      const u2 zb = pk4(z);
      f4 den[4];
#pragma unroll
      for (int i = 0; i < 4; ++i) den[i] = f4{0.f, 0.f, 0.f, 0.f};
#pragma unroll
      for (int s = 0; s < 3; ++s) {
        u4 zf;
#pragma unroll
        for (int o = 0; o < 2; ++o) {
          zf[2 * o] = 2 * s + o < 5 ? __builtin_amdgcn_ds_bpermute(srcl[(2 * s + o) % 5], zb[0]) : 0;
          zf[2 * o + 1] = 2 * s + o < 5 ? __builtin_amdgcn_ds_bpermute(srcl[(2 * s + o) % 5], zb[1]) : 0;
        }
#pragma unroll
        for (int i = 0; i < 4; ++i) den[i] = mfma(zf, u4{pq[i][2 * s][0], pq[i][2 * s][1], pq[i][2 * s + 1][0], pq[i][2 * s + 1][1]}, den[i]);
      }
      f4 sc;
#pragma unroll
      for (int i = 0; i < 4; ++i) sc[i] = __builtin_bit_cast(f2, gr[i])[0] * __builtin_amdgcn_rcpf(den[i][0] + 1e-6f);
      BS128(sc, rs, SC_OFF + ((rt * 4 + wm) * 32 + 8 * c + 4 * j) * 256 + vo);
      STAMP(0, 3);
      hv0 = flag_load(ws, hidx);
      VM0C(); VMCNT(0); BARRIER();
      flag_store(rs, tid, FL_S + rt * 4 + c, tag);
    } else {
      f4 acc[4][5];
      bf8 wf[2][5];
      mainloop<false, 5, 0>(acc, xf, wf, L, ask, []() {}, [](int) {}, [](int) {}, st1, st2);
      look();
      STAMP(0, 2); STAMPC(0, 7);
      constexpr int PA[8] = {0, 0, 1, 1, 1, 2, 2, 2}, PB[8] = {0, 1, 0, 1, 2, 1, 2, 0};
      u4 pk[2][3], vv[2][3];
      float et[16], pt[16];
      u32 ot[8];
#pragma unroll
      for (int b = 0; b < 6; ++b)
#pragma unroll
        for (int k = 0; k < 8; ++k) {
          if (b < 5) acc[k & 3][b % 5] = mfma(xf[k >> 2][k & 3], wf[k >> 2][b % 5], acc[k & 3][b % 5]);
          SB0();
          if (b > 0 && b < 4) {
            phi_stage(k, acc[0][b - 1], acc[1][b - 1], acc[2][b - 1], acc[3][b - 1], et, pt, ot);
            if (k == 7) { pk[0][b - 1] = u4{ot[0], ot[1], ot[2], ot[3]}; pk[1][b - 1] = u4{ot[4], ot[5], ot[6], ot[7]}; }
          }
          if (b >= 4) {
#pragma unroll
            for (int vb = (b == 4 ? 2 : 4); vb < (b == 4 ? 4 : 5); ++vb) {
              const u32 v = pk2(acc[k >> 1][vb][2 * (k & 1)], acc[k >> 1][vb][2 * (k & 1) + 1]);
              vv[k >> 2][vb - 2][k & 3] = vb == 2 ? pick(r16 == 0, 0x3f803f80u, v) : v;
            }
          }
#define K0 8
#define KPLAIN
          if (b < 4 && (k % 3 == 1) && 3 * b + k / 3 < K0) b_dma(3 * b + k / 3);
          if (b < 5) SB0();
        }
#pragma unroll
      for (int n = 0; n < 8; ++n) {
        const f4 kv = mfma(vv[1][PB[n]], pk[1][PA[n]], mfma(vv[0][PB[n]], pk[0][PA[n]], f4{0.f, 0.f, 0.f, 0.f}));
        *(LAS f4*)(w * 8192 + (w >> 2) * 98304 + n * 1024 + lane * 16) = kv;
      }
      LGKM0(); BARRIER();
      const u32 zo = pick((w == 0 || w == 2 || w == 7) & (lane < 32), ((w + 1) / 3) * 128 + lane * 4, OOB);
#pragma unroll
      for (int j2 = 0; j2 < 2; ++j2) {
        f4 sum = *(LAS f4*)((j2 * 8 + w) * 1024 + lane * 16);
#pragma unroll
        for (int m = 1; m < 4; ++m) sum += *(LAS f4*)(((2 * m + j2) * 8 + w) * 1024 + (m >> 1) * 98304 + lane * 16);
        BS128(sum, rs, KVP_OFF + ((rt * 16 + 2 * c + j2) * 8 + w) * 1024 + vo);
        const float z0 = sum[0];
        __builtin_amdgcn_raw_buffer_store_b32(__builtin_bit_cast(u32, z0), rs, zo, ZP_OFF + (rt * 16 + 2 * c + j2) * 384, SC1);
      }
      STAMP(0, 3);
      VM0C(); VMCNT(0); LGKM0(); BARRIER();
      flag_store(rs, tid, FL_KV + rt * 8 + c, tag);
      STAMP(1, 7);
#pragma unroll
      for (int k = K0; k < 20; ++k) b_dma(k);
      b_regs(0, 15);
      setup2();
      hv0 = flag_load(ws, hidx);
      VM0C(); VMCNT(0);
    }
  }
  STAMP(0, 4);

  u32 pcv[3] = {w * 4096 + vo, w * 4096 + vo + 2 * XBUF, w * 4096 + vo + 4 * XBUF}, vp = vo;
  asm volatile("" : "+v"(pcv[0]), "+v"(pcv[1]), "+v"(pcv[2]), "+v"(vp));
  auto pc_addr = [&](int k) { return pcv[pc_base(k) >> 16] + (pc_base(k) & 0xffff); };
  const u32 grow = GS_OFF + (rt2 * 64 + (w >> 1) * 16) * 8;
  u2 ge;
  {
#define OW 4
    constexpr int RA = 4;
    static_assert(OW == 4, "");
    f4 acc[4][5];
    u4 ar[RA][4], ao[2][4], bj[3], scl[2];
    u32 sa[10];
#pragma unroll
    for (int s = 0; s < 10; ++s) sa[s] = s < 7 ? pick(s == 6 && nh == 6, OOB, abase + (t0 + s) * 4096) : abase + (o0 + s - 7) * 4096;
    u4 ssrd = wssrd;
    ssrd[2] = SC_OFF + rt2 * 8192 + (ok1 / 20 + 1) * 256;
    auto lda1 = [&](int s, int i) { ar[s % RA][i] = BL128(rs, vp + i * 1024, sa[s]); };
    auto rds = [&](int s, int o) { scl[o] = *(LAS u4*)(pc_addr(16) - (vp & 0x300) + (((sco >> (4 * (2 * (s - 7) + o))) & 15) << 8)); };
    float slo, shi;
    auto scale1 = [&](int s, int q) {
      if (q > 0) {
        const int pi = (q - 1) >> 2, e = (q - 1) & 3;
        u32 r = pk2(slo, shi);
        asm volatile("" : "+v"(r));
        ao[s & 1][2 * (pi & 1) + (e >> 1)][2 * (pi >> 1) + (e & 1)] = r;
      }
      if (q < 16) {
        const int pi = q >> 2, e = q & 3;
        const float f = __builtin_bit_cast(f4, scl[pi >> 1])[2 * (pi & 1) + (e >> 1)];
        const u32 v = ar[s % RA][pi][e];
        slo = bflo(v) * f; shi = bfhi(v) * f;
        asm volatile("" : "+v"(slo), "+v"(shi));
      }
    };
    auto rdb = [&](int f) {
      if (f < 20) bj[f % 3] = *(LAS u4*)pc_addr(f);
      else if (f >= 35 && f < 50) {
        const int js = (f - 35) / 5, n = f % 5;
        const u32 a = n < 4 ? pc_addr(4 * js + (n >> 1)) + 8 * (n & 1) : pc_addr(12 + js) - (vp >> 1);
        const u2 lo = *(LAS u2*)a, hi = *(LAS u2*)(a + (n < 4 ? 2048 : 512));
        bj[f % 3] = u4{lo[0], lo[1], hi[0], hi[1]};
      }
    };
    auto oload = [&](int q) {
      if (q < 2) dma16n(q, pc_base(16) + w * 4096, vp, ssrd, SC_OFF + rt2 * 8192 + hb * 256);
      else {
        const int js = (q - 2) / 5, pp = (q - 2) % 5;
        dma16n(pp & 3, pc_base(pp < 4 ? 4 * js : 12 + js) + w * 4096, vp, wssrd, bbase + (o0 + js) * 5120 + (pp & 4) * 1024);
      }
    };
    rdb(0); rdb(1);
    if (w == 0) wait_flags(ws, hidx, tag, hv0);
    BARRIER();
    STAMP(1, 0);
#pragma unroll
    for (int s = 0; s < RA - 1; ++s) {
#pragma unroll
      for (int i = 0; i < 4; ++i) lda1(s, i);
      SB0();
    }
    ge = BL64(rs, (vp & 0xf0) >> 1, grow);
    MEMB(); SB0();
#pragma unroll
    for (int q = 0; q < 4; ++q) unpair1(ar[0], q);
    asm volatile("s_nop 1");
    u32 fl = 0;
    auto step = [&](auto S, auto LAST) __attribute__((always_inline)) {
      constexpr int s = S.v;
      constexpr bool last = LAST.v;
      if (s == OW) { wait_flags(ws, oidx, tag, fl); STAMP(1, 2); }
      SB0();
#pragma unroll
      for (int k = 0; k < 20; ++k) {
        const int n = k >> 2, i = k & 3;
        const u4 bop = (s >= 4 && s < 7) ? breg[(s + 2) % 3][n] : bj[(5 * s + n) % 3];
        mma_in(acc[i][n], bop, s < 7 ? ar[s % RA][opix(i)] : ao[s & 1][i], s == 0);
        SB0();
        if (s == 6 && k == 0) { VM0C(); VMCNT(0); }
        if (s == 7 && k == 11)
          asm volatile("s_waitcnt vmcnt(4)\n\ts_cmp_eq_u32 %0, 3\n\ts_cbranch_scc1 1f\n\ts_waitcnt vmcnt(0)\n1:" ::"s"(no) : "memory", "scc");
        if (s == 8 && !last && k == 0) asm volatile("" : "+v"(ar[1][0]), "+v"(ar[1][1]), "+v"(ar[1][2]), "+v"(ar[1][3])::"memory");
        if (s == 8 && !last && k == 11) VMCNT(0);
        if (i == 0 && !(last && n >= 3)) rdb(5 * s + n + 2);
        if (!last && s >= 6 && k == 0) { rds(s + 1, 0); rds(s + 1, 1); }
        const int sn = s + RA - 1;
        if (s != 5 && k % 5 == 1 && sn < 9) lda1(sn, k / 5);
        if (k == 3 && s == OW - 1) fl = flag_load(ws, oidx);
        if (s == 4 && k % 5 == 3) oload(k / 5);
        if (s == 5 && k < 14 && !(k & 1)) { if (k & 2) oload(4 + k / 4); else lda1(8, k / 4); }
        if (s == 6 && k % 4 == 1) oload(7 + k / 4);
        if (s == 6 && k % 4 == 3 && k < 16)
          asm volatile("s_cmp_eq_u32 %4, 2\n\ts_cbranch_scc1 1f\n\tbuffer_load_dwordx4 %0, %1, %2, %3 offen offset:%5 sc1\n1:"
                       : "=&v"(ar[1][(k / 4) & 3]) : "v"(vp), "s"(wssrd), "s"(sa[9]), "s"(no), "i"(((k / 4) & 3) * 1024) : "memory", "scc");
        if (s == 7 && k % 3 == 1 && k < 15) {
          const int q = k / 3;
          asm volatile("s_cmp_eq_u32 %4, 2\n\ts_cbranch_scc1 1f\n\ts_mov_b32 m0, %0\n\ts_nop 0\n\tbuffer_load_dwordx4 %1, %2, %3 offen offset:%5 sc1 lds\n1:"
                       ::"s"(pc_base(q < 4 ? 8 : 14) + w * 4096), "v"(vp), "s"(wssrd), "s"(bbase + (o0 + 2) * 5120 + (q < 4 ? 0 : 4096)), "s"(no), "i"((q & 3) * 1024)
                       : "memory", "scc");
        }
        MEMB();
        if (!last) {
          if (s < 6 && k >= 12 && k < 16) unpair1(ar[(s + 1) % RA], k - 12);
          if (s >= 6 && k >= 1 && k < 18) scale1(s + 1, k - 1);
        } else if (n >= 1) *(LAS f4*)pc_addr(((0x31240 >> (4 * (n - 1))) & 15) * 4 + i) = acc[i][n - 1];
        SB0();
      }
      if (last || s == 7 || s == OW - 1) asm volatile("s_nop 7" ::: "memory");
      SB0();
      if (last) {
#pragma unroll
        for (int i = 0; i < 4; ++i) *(LAS f4*)pc_addr(12 + i) = acc[i][4];
      }
    };
    step(IC<0>{}, IC<0>{}); step(IC<1>{}, IC<0>{}); step(IC<2>{}, IC<0>{}); step(IC<3>{}, IC<0>{}); step(IC<4>{}, IC<0>{});
    step(IC<5>{}, IC<0>{}); step(IC<6>{}, IC<0>{}); step(IC<7>{}, IC<0>{});
    if (no == 2) step(IC<8>{}, IC<1>{});
    int no2 = no;
    asm volatile("" : "+s"(no2));
    if (no2 == 3) { step(IC<8>{}, IC<0>{}); step(IC<9>{}, IC<1>{}); }
    STAMP(1, 3);
  }
  for (u32 spin = 0; !__all(ge[1] == tag) && spin < SPIN_CAP; ++spin) {
    __builtin_amdgcn_s_sleep(2);
    MEMB();
    ge = BL64(rs, (vp & 0xf0) >> 1, grow);
  }
  const float om = __builtin_amdgcn_rcpf(1.f + __builtin_bit_cast(f2, ge)[0]);
  STAMP(1, 4);
  LGKM0(); BARRIER();
  {
    const int i = w >> 1, n0 = (w & 1) * 3;
    u16* op = out + (rt2 * 64 + i * 16 + ((vp >> 4) & 15)) * D_ + ct * 80 + n0 * 16 + ((vp >> 8) << 2);
#pragma unroll
    for (int n = 0; n < 3; ++n) {
      if (n0 + n < 5) {
        const u32 a = ((0x43201u >> (4 * (n0 + n))) & 15) * XBUF + i * 1024 + vp;
        f4 v = *(LAS f4*)a;
#pragma unroll
        for (int s = 1; s < 8; ++s) v += *(LAS f4*)(a + s * 4096);
        *(u2*)(op + n * 16) = pk4(v * om);
      }
    }
  }
  STAMP(1, 5);
}

extern "C" void kernel_launch(void* const* d_in, const int* in_sizes, int n_in, void* d_out, int out_size, void* d_ws, size_t ws_size,
                              hipStream_t stream) {
  (void)in_sizes; (void)n_in; (void)out_size; (void)ws_size;
  (void)hipFuncSetAttribute((const void*)fused, hipFuncAttributeMaxDynamicSharedMemorySize, LDS_BYTES);
  fused<<<256, 512, LDS_BYTES, stream>>>((const u16*)d_in[0], (const u16*)d_in[1], (const u16*)d_in[2], (const u16*)d_in[4], (const u16*)d_in[5],
                                         (const u16*)d_in[6], (const u16*)d_in[7], (char*)d_ws, (u16*)d_out);
}
```

```cpp
#include <hip/hip_runtime.h>
#include <stdint.h>

typedef unsigned short u16;
typedef unsigned int u32;
typedef unsigned long long u64;
typedef short s4 __attribute__((ext_vector_type(4)));
typedef __bf16 bf8 __attribute__((ext_vector_type(8)));
typedef __bf16 bf2 __attribute__((ext_vector_type(2)));
typedef float f2 __attribute__((ext_vector_type(2)));
typedef float f4 __attribute__((ext_vector_type(4)));
typedef u32 u4 __attribute__((ext_vector_type(4)));
typedef u32 u2 __attribute__((ext_vector_type(2)));
#define LAS __attribute__((address_space(3)))
#define GAS __attribute__((address_space(1)))
#define DEV __device__ __forceinline__
#define SB0() __builtin_amdgcn_sched_barrier(0)

constexpr int S_ = 2048, D_ = 640, F_ = 1728;
constexpr int KS2 = 74, KH = 54;
constexpr u32 AF_OFF = 0, AF_BYTES = 32u * KS2 * 4096u;
constexpr u32 BF_OFF = AF_OFF + AF_BYTES, BF_BYTES = 8u * KS2 * 5120u;
constexpr u32 KVP_OFF = BF_OFF + BF_BYTES, KVP_BYTES = 8u * 16u * 8192u;
constexpr u32 ZP_OFF = KVP_OFF + KVP_BYTES, ZP_BYTES = 8u * 16u * 384u;
constexpr u32 SC_OFF = ZP_OFF + ZP_BYTES, SC_BYTES = 32u * 32u * 256u;
constexpr u32 GS_OFF = SC_OFF + SC_BYTES, GS_BYTES = 2048u * 8u;
constexpr u32 FLAG_OFF = GS_OFF + GS_BYTES, FLAG_BYTES = 561u * 128u;
constexpr int FL_R = 0, FL_G = 256, FL_KV = 400, FL_Q = 464, FL_M = 496, FL_TA = 512, FL_S = 528, FL_EPOCH = 560;
constexpr u32 WS_BYTES = FLAG_OFF + FLAG_BYTES;
#define STAMP(k, i) do {} while (0)
#define STAMPC(k, i) do {} while (0)

constexpr u32 XBUF = 32768, WRING = 3 * XBUF, WSLOT = 12288, WPAD = 8192, LDS_BYTES = 163840;
constexpr u32 RPAD = WRING + 2 * WSLOT, SPARE = WRING + XBUF + 2 * WSLOT;
constexpr u32 OOB = 0x40000000u;
constexpr int SC1 = 16;
typedef __amdgpu_buffer_rsrc_t rsrc_t;
#define BL128(rs, vo, so) __builtin_amdgcn_raw_buffer_load_b128(rs, vo, so, SC1)
#define BL64(rs, vo, so) __builtin_amdgcn_raw_buffer_load_b64(rs, vo, so, SC1)
#define BS128(v, rs, off) __builtin_amdgcn_raw_buffer_store_b128(__builtin_bit_cast(u4, v), rs, off, 0, SC1)
#define RLX_AGENT __ATOMIC_RELAXED, __HIP_MEMORY_SCOPE_AGENT

#define E5
#define NOCODE
#define CODEWG 248
#define RINGSC "sc1"
DEV u4 make_srd(const void* p, u32 bytes) {
  u64 a = (u64)p;
  u4 r = {(u32)a, (u32)(a >> 32) & 0xffffu, bytes, 0x00020000u};
  return r;
}
DEV void dma16(u32 lds, u32 voff, u4 srd, u32 soff) {
  asm volatile("s_mov_b32 m0, %0\n\ts_nop 0\n\tbuffer_load_dwordx4 %1, %2, %3 offen lds" ::"s"(lds), "v"(voff), "s"(srd), "s"(soff) : "memory");
}
template <int OFF> DEV void dma16o(u32 lds, u32 voff, u4 srd, u32 soff) {
  asm volatile("s_mov_b32 m0, %0\n\ts_nop 0\n\tbuffer_load_dwordx4 %1, %2, %3 offen offset:%4 " RINGSC " lds" ::"s"(lds), "v"(voff), "s"(srd), "s"(soff), "i"(OFF) : "memory");
}
DEV void dma16n(int d, u32 lds, u32 voff, u4 srd, u32 soff) {
  if (d == 0) dma16o<0>(lds, voff, srd, soff);
  else if (d == 1) dma16o<1024>(lds, voff, srd, soff);
  else if (d == 2) dma16o<2048>(lds, voff, srd, soff);
  else dma16o<3072>(lds, voff, srd, soff);
}
DEV void dma16c(u32 lds, u32 voff, u4 srd, u32 soff) {
  asm volatile("s_mov_b32 m0, %0\n\ts_nop 0\n\tbuffer_load_dwordx4 %1, %2, %3 offen sc1 lds" ::"s"(lds), "v"(voff), "s"(srd), "s"(soff) : "memory");
}
DEV void dma4c(u32 lds, u32 voff, u4 srd, u32 soff) {
  asm volatile("s_mov_b32 m0, %0\n\ts_nop 0\n\tbuffer_load_dword %1, %2, %3 offen sc1 lds" ::"s"(lds), "v"(voff), "s"(srd), "s"(soff) : "memory");
}
DEV s4 trread(u32 a) { return __builtin_amdgcn_ds_read_tr16_b64_v4i16((LAS s4*)a); }
DEV bf8 cat8(s4 lo, s4 hi) { return __builtin_bit_cast(bf8, __builtin_shufflevector(lo, hi, 0, 1, 2, 3, 4, 5, 6, 7)); }
DEV f4 mfma(bf8 a, bf8 b, f4 c) { return __builtin_amdgcn_mfma_f32_16x16x32_bf16(a, b, c, 0, 0, 0); }
DEV f4 mfma(u4 a, u4 b, f4 c) { return mfma(__builtin_bit_cast(bf8, a), __builtin_bit_cast(bf8, b), c); }
DEV u32 pk2(float a, float b) { f2 v = {a, b}; return __builtin_bit_cast(u32, __builtin_convertvector(v, bf2)); }
DEV u2 pk4(f4 v) { u2 r = {pk2(v[0], v[1]), pk2(v[2], v[3])}; return r; }
DEV u4 pk8(f4 a, f4 b) { const u2 x = pk4(a), y = pk4(b); return u4{x[0], x[1], y[0], y[1]}; }
DEV u32 pick(bool c, u32 a, u32 b) { return c ? a : b; }
DEV float bflo(u32 v) { return __builtin_bit_cast(float, v << 16); }
DEV float bfhi(u32 v) { return __builtin_bit_cast(float, v & 0xffff0000u); }
constexpr float LOG2E = 1.44269504089f;
DEV void phi_stage(int st, f4 t0, f4 t1, f4 t2, f4 t3, float (&e)[16], float (&p)[16], u32 (&o)[8]) {
  const f4 t[4] = {t0, t1, t2, t3};
#pragma unroll
  for (int q = 0; q < 16; ++q) {
    const float tq = t[q >> 2][q & 3];
    if (st == (q >> 3)) e[q] = tq * LOG2E;
    if (st == 1 + (q >> 2)) e[q] = __builtin_amdgcn_fmed3f(__builtin_amdgcn_exp2f(e[q]), 0.f, 1.f);
    if (st == (q < 4 ? 0 : 1 + (q >> 2))) p[q] = __builtin_amdgcn_fmed3f(tq, 1e-6f, __builtin_inff());
    if (st == 5 + (q >> 3)) { e[q] += p[q]; asm volatile("" : "+v"(e[q])); }
    if (st == 7 && (q & 1)) o[q >> 1] = pk2(e[q - 1], e[q]);
  }
}
DEV u4 glu_packed(f4 g0, f4 g1, f4 u0, f4 u1) {
  f2 e[4] = {f2{g0[0], g0[1]}, f2{g0[2], g0[3]}, f2{g1[0], g1[1]}, f2{g1[2], g1[3]}};
  const f2 u[4] = {f2{u0[0], u0[1]}, f2{u0[2], u0[3]}, f2{u1[0], u1[1]}, f2{u1[2], u1[3]}};
  u4 o;
#pragma unroll
  for (int q = 0; q < 4; ++q) e[q] *= -LOG2E;
#pragma unroll
  for (int q = 0; q < 4; ++q) e[q] = f2{__builtin_amdgcn_exp2f(e[q][0]), __builtin_amdgcn_exp2f(e[q][1])};
#pragma unroll
  for (int q = 0; q < 4; ++q) e[q] += 1.f;
#pragma unroll
  for (int q = 0; q < 4; ++q) e[q] = f2{__builtin_amdgcn_rcpf(e[q][0]), __builtin_amdgcn_rcpf(e[q][1])};
#pragma unroll
  for (int q = 0; q < 4; ++q) { e[q] *= u[q]; o[q] = pk2(e[q][0], e[q][1]); }
  return o;
}
DEV void glu_stage(int st, f4 g0, f4 g1, f4 u0, f4 u1, float (&e)[8], u4& o) {
  const f4 gg[2] = {g0, g1}, uu[2] = {u0, u1};
#pragma unroll
  for (int q = 0; q < 8; ++q) {
    if (st == 0) { e[q] = gg[q >> 2][q & 3] * -LOG2E; asm volatile("" : "+v"(e[q])); }
    if (st == 1 + (q >> 2)) e[q] = __builtin_amdgcn_exp2f(e[q]);
    if (st == 3) { e[q] += 1.f; asm volatile("" : "+v"(e[q])); }
    if (st == 4 + (q >> 2)) e[q] = __builtin_amdgcn_rcpf(e[q]);
    if (st == 6) { e[q] *= uu[q >> 2][q & 3]; asm volatile("" : "+v"(e[q])); }
    if (st == 7 && (q & 1)) o[q >> 1] = pk2(e[q - 1], e[q]);
  }
}
#define MEMB() asm volatile("" ::: "memory")
#define VM0C() __builtin_amdgcn_s_waitcnt(0x0F70)
#define SGB(mask, n) __builtin_amdgcn_sched_group_barrier(mask, n, 0)
#define LGKM0() __builtin_amdgcn_s_waitcnt(0xC07F)
#define VMCNT(n) asm volatile("s_waitcnt vmcnt(" #n ")" ::: "memory")
#define BARRIER() do { asm volatile("" ::: "memory"); __builtin_amdgcn_s_barrier(); asm volatile("" ::: "memory"); } while (0)

DEV u32 flag_load(char* ws, int idx) {
  asm volatile("" ::: "memory");
  return __builtin_amdgcn_raw_buffer_load_b32(__builtin_amdgcn_make_buffer_rsrc(ws, 0, WS_BYTES, 0x00020000), idx * 128, FLAG_OFF, SC1);
}
DEV void flag_store(__amdgpu_buffer_rsrc_t rs, u32 id, int idx, u32 v) {
  __builtin_amdgcn_raw_buffer_store_b32(v, rs, id < 32 ? id * 4 : OOB, FLAG_OFF + idx * 128, SC1);
  asm volatile("" ::: "memory");
}
constexpr u32 SPIN_CAP = 1u << 16;
DEV void wait_flags(char* ws, int idx, u32 tag, u32 v) {
#define PSLEEP 2
  for (u32 spin = 0; !__all(v == tag) && spin < SPIN_CAP; ++spin) {
    __builtin_amdgcn_s_sleep(PSLEEP);
    v = flag_load(ws, idx);
    asm volatile("" : "+v"(v));
  }
  asm volatile("" ::: "memory");
}
DEV u32 load_epoch(char* ws) {
  u32 e;
  asm volatile("s_load_dword %0, %1, 0x0\n\ts_waitcnt lgkmcnt(0)" : "=s"(e) : "s"(ws + FLAG_OFF + FL_EPOCH * 128) : "memory");
  return e;
}
DEV void gate_load(rsrc_t rs, int row0, int r16, u2 (&gr)[4]) {
#pragma unroll
  for (int i = 0; i < 4; ++i) gr[i] = BL64(rs, r16 * 8 + i * 128, GS_OFF + row0 * 8);
}
DEV void gate_wait(rsrc_t rs, int row0, int r16, u32 tag, u2 (&gr)[4]) {
  for (u32 spin = 0; !__all(gr[0][1] == tag && gr[1][1] == tag && gr[2][1] == tag && gr[3][1] == tag) && spin < SPIN_CAP; ++spin) {
    __builtin_amdgcn_s_sleep(2);
    asm volatile("" ::: "memory");
    gate_load(rs, row0, r16, gr);
    asm volatile("" : "+v"(gr[0]), "+v"(gr[1]), "+v"(gr[2]), "+v"(gr[3]));
  }
}

struct Loop {
  u4 xsrd, wsrd;
  u32 xso, wso, wpitch64;
  u32 xvo[4], wvo[3];
  u32 xdst, wdst;
  u32 xrd[2], wrd[5];
};
struct St { u32 xa, wa[5], rx, dw, dx, sw, sx; };
template <int NB> DEV u32 waddr(const u32 (&wa)[5], int b) { return NB == 6 ? wa[b % 3] + (b / 3) * 6144 : wa[b]; }

template <bool TR, int NB, int H, int MODE, class FN>
DEV void half_step(f4 (&acc)[4][NB], const bf8 (&xc)[4], const bf8 (&wc)[NB], bf8 (&xn)[4], bf8 (&wn)[NB], const Loop& L, St& s, FN fn) {
  constexpr int NA = NB == 6 ? 3 : 5;
  s4 lo[NB];
#pragma unroll
  for (int k = 0; k < 4 * NB; ++k) {
    const int b = k >> 2, i = k & 3;
    acc[i][b] = TR ? mfma(wc[b], xc[i], acc[i][b]) : mfma(xc[i], wc[b], acc[i][b]);
    SB0();
    if (MODE < 2) {
      if (k < 4) xn[k] = *(LAS bf8*)(s.xa + k * 2048);
      else if (k < 4 + 2 * NB) {
        const int nb = (k - 4) >> 1;
        const u32 a = waddr<NB>(s.wa, nb) + (H == 0 ? WSLOT : 0);
        if (k & 1) wn[nb] = cat8(lo[nb], trread(a + 768)); else lo[nb] = trread(a);
      }
      if (k == 4) {
        if (H == 0) {
          asm volatile("" : "+s"(s.rx));
          s.rx = s.rx == 2 * XBUF ? 0 : s.rx + XBUF;
        }
        s.xa = L.xrd[H] + s.rx;
        asm volatile("" : "+v"(s.xa));
      }
      if (H == 0 && k >= 4 + 2 * NB && k < 4 + 2 * NB + NA) {
        s.wa[k - 4 - 2 * NB] ^= WRING ^ (WRING + 2 * WSLOT + WPAD);
        asm volatile("" : "+v"(s.wa[k - 4 - 2 * NB]));
      }
    }
    if (MODE == 0) {
#define WSP 3
#define XSP NB
#define XOF 1
      if (H == 1 && k % WSP == 1 % WSP && (k - 1) / WSP < 3 && k >= 1) dma16n((k - 1) / WSP, s.dw, L.wvo[(k - 1) / WSP], L.wsrd, s.sw);
      if (H == 0 && k >= XOF && (k - XOF) % XSP == 0 && (k - XOF) / XSP < 4) dma16n((k - XOF) / XSP, s.dx, L.xvo[(k - XOF) / XSP], L.xsrd, s.sx);
      if (k == 2) {
        if (H == 0) {
          asm volatile("" : "+s"(s.dw), "+s"(s.sw));
          s.dw ^= WRING ^ (WRING + 2 * WSLOT + WPAD); s.sw += L.wpitch64;
          asm volatile("" : "+s"(s.dw), "+s"(s.sw));
        } else {
          asm volatile("" : "+s"(s.dx), "+s"(s.sx));
          s.dx = s.dx >= 2 * XBUF ? s.dx - 2 * XBUF : s.dx + XBUF; s.sx += 128;
          asm volatile("" : "+s"(s.dx), "+s"(s.sx));
        }
      }
    }
    fn(k);
    SB0();
  }
}

template <bool TR, int NB, int N8A, class ASK, class LOOK, class F0, class F1, class ST, class ST2>
DEV void mainloop(f4 (&acc)[4][NB], bf8 (&xf)[2][4], bf8 (&wf)[2][NB], const Loop& L, ASK ask, LOOK look, F0 f8a, F1 f8b, ST st, ST2 st2) {
  constexpr int NA = NB == 6 ? 3 : 5;
  bf8 x1[4], w1[NB];
#pragma unroll
  for (int i = 0; i < 4; ++i)
#pragma unroll
    for (int b = 0; b < NB; ++b) { acc[i][b] = f4{0.f, 0.f, 0.f, 0.f}; asm volatile("" : "+v"(acc[i][b])); }
  St s;
  s.rx = 0; s.xa = L.xrd[1];
#pragma unroll
  for (int b = 0; b < NA; ++b) s.wa[b] = L.wrd[b];
  s.dw = L.wdst ^ (WRING ^ (WRING + 2 * WSLOT + WPAD)); s.sw = L.wso + L.wpitch64;
  s.dx = 2 * XBUF + L.xdst; s.sx = L.xso + 2 * 128;
  SB0();
  VMCNT(9);
  BARRIER();
  st();
#pragma unroll
  for (int i = 0; i < 4; ++i) xf[0][i] = *(LAS bf8*)(L.xrd[0] + i * 2048);
#pragma unroll
  for (int b = 0; b < NB; ++b) { const u32 a = waddr<NB>(L.wrd, b); wf[0][b] = cat8(trread(a), trread(a + 768)); }
  auto nop = [](int) {};
#pragma unroll 1
  for (int t = 0; t < 8; ++t) {
    SB0(); LGKM0(); SB0();
    half_step<TR, NB, 0, 0>(acc, xf[0], wf[0], x1, w1, L, s, nop);
    SB0(); VMCNT(4); LGKM0(); BARRIER(); SB0();
    half_step<TR, NB, 1, 0>(acc, x1, w1, xf[0], wf[0], L, s, [&](int k) { ask(t, k); });
  }
  look();
  SB0(); LGKM0(); SB0();
  half_step<TR, NB, 0, 1>(acc, xf[0], wf[0], x1, w1, L, s, f8a);
  SB0();
  asm volatile("s_waitcnt vmcnt(%0)" ::"i"(N8A) : "memory");
  LGKM0(); BARRIER(); SB0();
  s4 lo[NB];
  half_step<TR, NB, 1, 1>(acc, x1, w1, xf[0], wf[0], L, s, [&](int k) {
    const int r = k - 2;
    if (r >= 0 && r < 2 * NB) {
      const u32 a = waddr<NB>(s.wa, r >> 1) + WSLOT;
      if (r & 1) wf[1][r >> 1] = cat8(lo[r >> 1], trread(a + 768)); else lo[r >> 1] = trread(a);
    } else if (r >= 2 * NB && r < 2 * NB + 4) xf[1][r - 2 * NB] = *(LAS bf8*)(L.xrd[1] + (r - 2 * NB) * 2048);
    f8b(k);
  });
  SB0(); LGKM0(); BARRIER(); SB0();
}

DEV constexpr u32 pc_base(int k) { return (k < 4 ? XBUF : k < 8 ? WRING : k < 12 ? 2 * XBUF : k < 16 ? WRING + XBUF : 0) + (k & 3) * 1024; }

DEV void unpair1(u4 (&p)[4], int q) {
  const int ip = q >> 1, e = q & 1;
  u32 x = p[ip][2 + e], y = p[2 + ip][e];
  asm volatile("v_swap_b32 %0, %1" : "+v"(x), "+v"(y));
  p[ip][2 + e] = x; p[2 + ip][e] = y;
}
DEV void mma_in(f4& c, u4 a, u4 b, bool first) {
  if (first) asm volatile("v_mfma_f32_16x16x32_bf16 %0, %1, %2, 0" : "=&v"(c) : "v"(a), "v"(b));
  else asm volatile("v_mfma_f32_16x16x32_bf16 %0, %1, %2, %0" : "+v"(c) : "v"(a), "v"(b));
}
DEV constexpr int opix(int i) { return 2 * (i & 1) + (i >> 1); }
template <int V> struct IC { static constexpr int v = V; };

__global__ __launch_bounds__(512, 2) void fused(const u16* __restrict__ x, const u16* __restrict__ wqkv, const u16* __restrict__ wo,
                                                const u16* __restrict__ wg, const u16* __restrict__ wu, const u16* __restrict__ wd,
                                                const u16* __restrict__ gw, char* ws, u16* __restrict__ out) {
  extern __shared__ __attribute__((aligned(16))) char smem[];
  int lane = threadIdx.x & 63, w = __builtin_amdgcn_readfirstlane(threadIdx.x >> 6), bid = blockIdx.x;
  asm volatile("" ::"s"(x), "s"(wqkv), "s"(wo), "s"(wg), "s"(wu), "s"(wd), "s"(gw), "s"(ws), "s"(out));
  Loop L;
  int rt = 0, cq = 0, type = 0, c = 0;
  STAMP(0, 0);
  if (bid < 240) {
    const int xcd = bid & 7, jj = bid >> 3;
    const int idx = (xcd >> 1) * 30 + jj;
    rt = (xcd & 1) * 4 + (idx & 3); cq = idx >> 2;
    L.xsrd = make_srd(x, S_ * D_ * 2);
    L.xso = (rt * 256 + w * 32) * 1280;
#pragma unroll
    for (int i = 0; i < 4; ++i) L.xvo[i] = (lane >> 3) * 1280 + (((lane & 7) ^ ((4 * i + (lane >> 4)) & 7)) << 4) + i * (10240 - 1024);
    L.xdst = w * 4096;
    asm volatile("s_nop 4");
#pragma unroll
    for (int i = 0; i < 4; ++i) dma16n(i, L.xdst, L.xvo[i], L.xsrd, L.xso);
    asm volatile("" : "+s"(cq), "+s"(w)); asm volatile("" : "+v"(lane));
    __builtin_assume(w >= 0 && w < 8 && lane >= 0 && lane < 64 && cq >= 0 && cq < 30);
    const int c15 = cq >= 15 ? cq - 15 : cq, up = cq >= 15;
    const int r8 = c15 & 7, h8 = c15 >> 3;
    type = (0x02100100 >> (4 * r8)) & 15;
    const int ord = (0x40132010 >> (4 * r8)) & 15;
    c = up * pick(type == 0, 9, 4 >> ((type - 1) & 1)) + h8 * pick(type == 0, 5, 2 >> ((type - 1) & 1)) + ord;
    {
      const int part = w >> 2, wq = w & 3;
      const u32 pitch = type == 0 ? F_ * 2 : 3 * D_ * 2;
      const u32 colbase = pick(type == 0, 96 * c, pick(type == 1, 640 + 640 * part + 80 * c, 160 * c + 80 * part));
      const int nch = type == 0 ? 12 : 10;
      L.wsrd = make_srd(type == 0 ? (part ? wu : wg) : wqkv, D_ * pitch);
      L.wpitch64 = 64 * pitch;
      L.wso = 16 * wq * pitch + colbase * 2;
#pragma unroll
      for (int i = 0; i < 3; ++i) {
        const int n = i * 64 + lane, row = n / 12, ch = (n % 12) ^ (((row >> 3) & 1) << 1);
        L.wvo[i] = pick(ch < nch, row * pitch + (ch << 4) - i * 1024, OOB);
      }
      L.wdst = WRING + (wq >> 1) * WSLOT + part * 6144 + (wq & 1) * 3072;
    }
    asm volatile("s_nop 4");
#pragma unroll
    for (int i = 0; i < 3; ++i) dma16n(i, L.wdst, L.wvo[i], L.wsrd, L.wso);
    BARRIER();
#pragma unroll
    for (int i = 0; i < 4; ++i) dma16n(i, XBUF + L.xdst, L.xvo[i], L.xsrd, L.xso + 128);
#pragma unroll
    for (int i = 0; i < 3; ++i) dma16n(i, L.wdst + XBUF, L.wvo[i], L.wsrd, L.wso + L.wpitch64);
  }
  asm volatile("" : "+s"(bid), "+s"(w)); asm volatile("" : "+v"(lane));
  __builtin_assume(w >= 0 && w < 8 && lane >= 0 && lane < 64 && bid >= 0 && bid < 256);
  const int tid = w * 64 + lane, xcd = bid & 7, jj = bid >> 3;
  const int g = lane >> 4, r16 = lane & 15;
  const rsrc_t rs = __builtin_amdgcn_make_buffer_rsrc(ws, 0, WS_BYTES, 0x00020000);
  const u4 wssrd = make_srd(ws, WS_BYTES);
  const u32 vo = lane * 16;
#define MAP2 1
  const int rt2 = MAP2 == 0 ? (xcd >> 1) * 8 + (jj >> 2) : xcd * 4 + (jj >> 3), ct = MAP2 == 0 ? (xcd & 1) * 4 + (jj & 3) : (jj & 7);
  const u32 abase = AF_OFF + rt2 * (KS2 * 4096u), bbase = BF_OFF + ct * (KS2 * 5120u);
  const int t0 = 7 * w - (w == 7), nh = 7 - (w >= 6), o0 = KH + pick(w < 4, 3 * w, 2 * w + 4), no = 2 + (w < 4);
  u4 breg[3][5];
  auto b_dma = [&](int k) { dma16n(k & 3, pc_base(k & ~3) + w * 4096, vo, wssrd, bbase + t0 * 5120 + (k & ~3) * 1024); };
  auto b_regs = [&](int k0, int k1) {
    const u32 sb = bbase + (t0 + 4) * 5120;
#pragma unroll
    for (int k = k0; k < k1; ++k) breg[k / 5][k % 5] = BL128(rs, vo + (k & 3) * 1024, pick(k >= 10 && nh == 6, OOB, sb + (k >> 2) * 4096));
  };
#define NBR 15
  auto b_reg_if = [&](int k, bool again) {
    const u32 so = pick(k >= 10 && nh == 6, OOB, bbase + (t0 + 4) * 5120 + (k >> 2) * 4096);
    if (!again)
      asm volatile("s_cmp_lg_u32 %4, 0\n\ts_cbranch_scc1 1f\n\tbuffer_load_dwordx4 %0, %1, %2, %3 offen offset:%5 sc1\n1:"
                   : "=&v"(breg[k / 5][k % 5]) : "v"(vo), "s"(wssrd), "s"(so), "s"(w), "i"((k & 3) * 1024) : "memory", "scc");
    else
      asm volatile("s_cmp_eq_u32 %4, 0\n\ts_cbranch_scc1 1f\n\tbuffer_load_dwordx4 %0, %1, %2, %3 offen offset:%5 sc1\n1:"
                   : "+v"(breg[k / 5][k % 5]) : "v"(vo), "s"(wssrd), "s"(so), "s"(w), "i"((k & 3) * 1024) : "memory", "scc");
  };
  const u4 wdsrd = make_srd(wd, F_ * D_ * 2);
  auto r_in = [&](int u, u32 lds) {
    const int row = 4 * w + g;
    dma16(lds + w * 1024, row * 1280 + ((r16 ^ ((row & 7) << 1)) << 4), wdsrd, (u / 5) * 40960 + (u % 5) * 256);
  };
  auto r_out = [&](int u, u32 lds) {
    const int row = 4 * g + (r16 >> 2), n40 = (u % 5) * 8 + w;
    const u32 a = lds + row * 256 + (((2 * w + ((lane >> 1) & 1)) ^ ((row & 7) << 1)) << 4) + 8 * (lane & 1);
    BS128(cat8(trread(a), trread(a + 4096)), rs, vo + BF_OFF + (((n40 / 5) * KS2 + u / 5) * 5 + n40 % 5) * 1024);
  };
  const int hidx = pick(w == 0, FL_G + (rt2 >> 2) * 18 + (lane < 18 ? lane : 17), OOB / 128);
  const int ok0 = (o0 - KH) * 32, ok1 = ok0 + no * 32 - 1;
  int oidx;
  auto setup2 = [&]() {
    u32 l = vo >> 4;
    asm volatile("" : "+v"(l));
    const int mc = ok0 / 40 + (l & 3), mc1 = ok1 / 40;
    oidx = pick(l < 4, FL_M + (mc < mc1 ? mc : mc1), FL_S + (rt2 >> 2) * 4 + pick(l & 1, ok0, ok1) / 160);
    asm volatile("" : "+v"(oidx));
  };
  u32 tag, hv0;
  if (jj >= 30) {
    const int tw = (jj - 30) * 8 + xcd;
    u32 so[5];
#pragma unroll
    for (int i = 0; i < 5; ++i) { const int n = i * 64 + lane, row = 4 * w + n / 80, p = n % 80; so[i] = row * 1280 + ((p ^ ((row & 7) << 1)) << 4); }
    const u4 wosrd = make_srd(wo, D_ * D_ * 2);
    asm volatile("s_nop 4");
#pragma unroll
    for (int i = 0; i < 8; ++i) r_in(144 + 16 * i + tw, 2 * XBUF + i * 8192);
    if (w < 5) {
#pragma unroll
      for (int s = 0; s < 2; ++s)
#pragma unroll
        for (int i = 0; i < 5; ++i) dma16(s * 25600 + w * 5120 + i * 1024, so[i], wosrd, (tw * 2 + s) * 25600);
    }
    tag = load_epoch(ws) + 1u;
    const int q = r16 >> 2, p = lane & 3;
    VMCNT(0); BARRIER();
#pragma unroll
    for (int i = 0; i < 8; ++i)
      if (i < 7 || tw < 14) r_out(144 + 16 * i + tw, 2 * XBUF + i * 8192);
    VM0C(); VMCNT(0); LGKM0(); BARRIER();
    flag_store(rs, tid, FL_R + bid, tag);
    STAMP(0, 1);
    u4 xv[16], xt[4], gv[2];
    {
      const u4* xp = (const u4*)(x + (tw * 128 + w * 16) * D_);
      gv[0] = ((const u4*)gw)[lane]; gv[1] = ((const u4*)gw)[64 + r16];
#pragma unroll
      for (int r = 0; r < 16; ++r) xv[r] = xp[r * 80 + lane];
#pragma unroll
      for (int i = 0; i < 4; ++i) xt[i] = xp[(4 * i + g) * 80 + 64 + r16];
    }
    {
      auto dot8 = [](u4 a, u4 b) {
        float s0 = 0.f;
#pragma unroll
        for (int e = 0; e < 4; ++e) { const u32 ae = a[e], be = b[e]; s0 = __builtin_amdgcn_fdot2_f32_bf16(__builtin_bit_cast(bf2, ae), __builtin_bit_cast(bf2, be), s0, false); }
        return s0;
      };
      float v[16];
#pragma unroll
      for (int r = 0; r < 16; ++r) v[r] = dot8(xv[r], gv[0]);
#pragma unroll
      for (int i = 0; i < 4; ++i) {
        const float tl = dot8(xt[i], gv[1]);
#pragma unroll
        for (int e = 0; e < 4; ++e) v[4 * i + e] += g == e ? tl : 0.f;
      }
#pragma unroll
      for (int b = 0; b < 4; ++b) {
        const bool hi = (lane >> b) & 1;
#pragma unroll
        for (int r = 0; r < (8 >> b); ++r) {
          const float keep = hi ? v[2 * r + 1] : v[2 * r], send = hi ? v[2 * r] : v[2 * r + 1];
          v[r] = keep + __shfl_xor(send, 1 << b);
        }
      }
      float a0 = v[0];
      a0 += __shfl_xor(a0, 16); a0 += __shfl_xor(a0, 32);
      const float gate = __builtin_amdgcn_exp2f(LOG2E * __builtin_amdgcn_fmed3f(a0, -60.f, 60.f));
      __builtin_amdgcn_raw_buffer_store_b64(u2{__builtin_bit_cast(u32, gate), tag}, rs, pick(lane < 16, lane * 8, OOB), GS_OFF + (tw * 128 + w * 16) * 8, SC1);
    }
    STAMP(0, 5);
    if (w == 0) {
      u32 v[4];
#pragma unroll
      for (int k = 0; k < 4; ++k) v[k] = flag_load(ws, FL_R + k * 64 + lane);
#pragma unroll
      for (int k = 0; k < 4; ++k) wait_flags(ws, FL_R + k * 64 + lane, tag, v[k]);
      flag_store(rs, lane, FL_TA + tw, tag);
      flag_store(rs, lane, pick(tw == 0, FL_EPOCH, OOB / 128), tag);
      STAMP(1, 1);
    }
    bf8 wt[2][5];
#pragma unroll
    for (int s = 0; s < 2; ++s)
#pragma unroll
      for (int nb = 0; nb < 5; ++nb) {
        const int ch = 2 * (w * 5 + nb) + (p >> 1);
        s4 v[2];
#pragma unroll
        for (int t = 0; t < 2; ++t) {
          const int e0 = 8 * g + 4 * t, row = (e0 < 20 ? e0 : 0) + q;
          v[t] = trread(s * 25600 + row * 1280 + ((ch ^ ((row & 7) << 1)) << 4) + 8 * (p & 1));
          if (e0 >= 20) v[t] = s4{0, 0, 0, 0};
        }
        wt[s][nb] = cat8(v[0], v[1]);
      }
    LGKM0(); BARRIER();
    *(LAS u2*)(tid * 8) = u2{0u, 0u};
    LGKM0(); BARRIER();
#pragma unroll
    for (int k = 0; k < 16; ++k) b_dma(k);
    if (w >= 2) { b_dma(16); b_dma(17); b_dma(18); b_dma(19); }
    b_regs(0, 15);
    const int dj = 16 * ((0x22211100 >> (4 * w)) & 15) + r16, ej = 16 * ((0x02121010 >> (4 * w)) & 15) + 4 * g - 8, hh = dj >= 20;
    u32 kdst = pick((dj < 40) & (ej >= 0) & ((ej >= 20) == hh), (dj + 16 * hh) * 64 + (ej - 20 * hh) * 2, 4096 + lane * 8), mdst[3], md16[3];
#pragma unroll
    for (int sb = 0; sb < 3; ++sb) {
      const int q4 = 10 * tw + 4 * sb + g;
      const u32 st = pick(sb < 2 || g < 2, BF_OFF + (w * KS2 + KH + (q4 >> 3)) * 5120u, OOB), hf = (q4 >> 2) & 1, ln = (q4 & 3) * 16 + r16;
      mdst[sb] = st + 4096 + hf * 512 + ln * 8; md16[sb] = st + hf * 2048 + ln * 16;
      asm volatile("" : "+v"(mdst[sb]), "+v"(md16[sb]));
    }
    asm volatile("" : "+v"(kdst));
    setup2();
    VM0C();
    if (w == 0) wait_flags(ws, FL_KV + (lane & 7) * 8 + (tw >> 1), tag, flag_load(ws, FL_KV + (lane & 7) * 8 + (tw >> 1)));
    BARRIER();
    STAMP(1, 6);
    {
      u4 pv[8];
#pragma unroll
      for (int r = 0; r < 8; ++r) pv[r] = BL128(rs, vo, pick(w < 7, KVP_OFF + ((r * 16 + tw) * 8 + w) * 1024, OOB));
      f4 sum = __builtin_bit_cast(f4, pv[0]);
#pragma unroll
      for (int r = 1; r < 8; ++r) sum += __builtin_bit_cast(f4, pv[r]);
      *(LAS u2*)kdst = pk4(sum);
    }
    LGKM0(); BARRIER();
    STAMP(0, 2);
    bf8 ka[4];
#pragma unroll
    for (int sb = 0; sb < 4; ++sb) ka[sb] = *(LAS bf8*)((sb * 16 + r16) * 64 + g * 16);
#pragma unroll
    for (int sb = 0; sb < 3; ++sb) {
      f4 m[5];
#pragma unroll
      for (int nb = 0; nb < 5; ++nb) {
        m[nb] = mfma(ka[sb == 2 ? 3 : sb], wt[sb >> 1][nb], f4{0.f, 0.f, 0.f, 0.f});
        if (sb == 1) m[nb] = mfma(ka[2], wt[1][nb], m[nb]);
      }
      BS128(pk8(m[0], m[1]), rs, md16[sb]);
      BS128(pk8(m[2], m[3]), rs, md16[sb] + 1024);
      __builtin_amdgcn_raw_buffer_store_b64(pk4(m[4]), rs, mdst[sb], 0, SC1);
    }
    STAMP(0, 3);
    hv0 = flag_load(ws, hidx);
    VM0C(); VMCNT(0); BARRIER();
    flag_store(rs, tid, FL_M + tw, tag);
    STAMP(1, 7);
    if (w < 2) { b_dma(16); b_dma(17); b_dma(18); b_dma(19); }
  } else {
    const int wm = w >> 1, j = w & 1;
    const int nk = pick(type == 0, rt * 18 + c, 1 << 16);
    r_in(nk, RPAD);
    SB0();
    {
#pragma unroll
      for (int s = 0; s < 2; ++s) L.xrd[s] = (64 * wm + r16) * 128 + (((4 * s + g) ^ (r16 >> 1)) << 4);
      const int q = r16 >> 2, p = lane & 3;
#pragma unroll
      for (int b = 0; b < 5; ++b) {
        const int u = 2 * b + (p >> 1);
        const int ch = pick(type == 0, 6 * j + u, pick(type == 1, 5 * j + u - 5 * (u >= 5), u));
        const int part = pick(type == 0, 0, pick(type == 1, u >= 5, j));
        L.wrd[b] = WRING + part * 6144 + (8 * g + q) * 192 + ((ch ^ ((g & 1) << 1)) << 4) + 8 * (p & 1);
      }
    }
#define NB1 4
#define TB2 2
    const int wq = w & 3;
    const bool early = w < 4;
    u32 tch, tso;
    u4 tsrd;
    {
      const int u = nk + 144 * wq, st = (early ? 2 : 6) + u / 84, r = u % 84;
      const u64 p0 = (u64)wqkv, p1 = (u64)wg, p2 = (u64)wu, p12 = r < 57 ? +p1 : +p2, pw = r < 30 ? +p0 : +p12, pp = wq < 3 ? +pw : (u64)x;
      tsrd = make_srd((const void*)pp, pick(wq == 3, S_ * D_ * 2, pick(u >= 336, 0, pick(r < 30, 3 * D_ * D_ * 2, F_ * D_ * 2))));
      const int li = nk * 57 + lane;
      tch = wq < 3 ? lane * 128 : pick((lane < 57) & (li < 8192), (li >> 2) * 1280 + ((early ? 2 : 6) + (li & 3)) * 128, OOB);
      tso = pick(wq < 3, pick(r < 30, st * 30 + r, st * 27 + r - pick(r < 57, 30, 57)) * 8192, 0);
      asm volatile("" : "+v"(tch));
    }
    const u32 twhen = pick(early || type != 0, 99, TB2);
    dma4c(SPARE + 2048 + w * 256, tch, tsrd, pick(early, tso, OOB));
    tag = load_epoch(ws) + 1u;
    auto st1 = [&]() { STAMP(0, 1); STAMPC(0, 6); };
    auto st2 = [&](int t) {
    };
    const u32 af = AF_OFF + (u32)(rt * 4 + wm) * (KS2 * 4096u) + vo;
    const u32 tadr = FLAG_OFF + (FL_TA + (jj & 1) * 8 + xcd) * 128;
    u32 rlds;
    {
      const int row = 4 * g + (r16 >> 2);
      rlds = RPAD + row * 256 + (((2 * w + ((lane >> 1) & 1)) ^ ((row & 7) << 1)) << 4) + 8 * (lane & 1);
      asm volatile("" : "+v"(rlds));
    }
    const int n40 = (nk % 5) * 8 + w;
    const u32 rdst = BF_OFF + (((n40 / 5) * KS2 + nk / 5) * 5 + n40 % 5) * 1024, hooks = pick(type != 0, 0x42, 0x43);
    u4 fsrd = wssrd;
    fsrd[2] = pick(w == 0, FLAG_OFF + (FL_R + bid + 1) * 128, 0u);
    auto ask = [&](int t, int k) {
      if (k == 0) {
        asm volatile("s_cmp_lg_u32 %0, %1\n\ts_cbranch_scc1 1f\n\ts_mov_b32 m0, %2\n\ts_nop 0\n\tbuffer_load_dword %3, %4, %5 offen sc1 lds\n1:"
                     ::"s"(t), "s"(twhen), "s"(SPARE + 2048 + w * 256), "v"(tch), "s"(tsrd), "s"(tso) : "memory", "scc");
        asm volatile(
            "s_bitcmp1_b32 %10, %0\n\t"
            "s_cbranch_scc0 3f\n\t"
            "s_cmp_eq_u32 %0, 1\n\t"
            "s_cbranch_scc1 1f\n\t"
            "s_cmp_eq_u32 %0, 6\n\t"
            "s_cbranch_scc1 2f\n\t"
            "ds_read_b64_tr_b16 v[252:253], %1\n\t"
            "ds_read_b64_tr_b16 v[254:255], %1 offset:4096\n\t"
            "s_waitcnt lgkmcnt(0)\n\t"
            "buffer_store_dwordx4 v[252:255], %2, %3, %4 offen sc1\n\t"
            "s_nop 1\n\t"
            "s_branch 3f\n"
            "1:\n\t"
            "v_mov_b32 v252, %7\n\t"
            "v_lshrrev_b32 v253, 2, %2\n\t"
            "buffer_store_dword v252, v253, %5, %6 offen sc1\n\t"
            "s_branch 3f\n"
            "2:\n\t"
            "s_mov_b32 m0, %8\n\t"
            "s_nop 0\n\t"
            "buffer_load_dword off, %3, %9 sc1 lds\n"
            "3:"
            :
            : "s"(t), "v"(rlds), "v"(vo), "s"(wssrd), "s"(rdst), "s"(fsrd), "s"(FLAG_OFF + (FL_R + bid) * 128), "s"(tag), "s"(SPARE + w * 256), "s"(tadr), "s"(hooks)
            : "memory", "scc", "v252", "v253", "v254", "v255");
      }
    };
    auto look = [&]() {
      u32 v = *(LAS u32*)(SPARE + w * 256);
      for (u32 spin = 0; __builtin_amdgcn_readfirstlane(v) != tag && spin < SPIN_CAP; ++spin) {
        __builtin_amdgcn_s_sleep(2);
        asm volatile("buffer_load_dword %0, off, %1, %2 sc1\n\ts_waitcnt vmcnt(0)" : "=v"(v) : "s"(wssrd), "s"(tadr) : "memory");
      }
    };
    auto f8a = [&](int k) { if (k % 5 == 1 && k < 20) b_dma(k / 5); };
    auto f8b = [&](int k) { if (k % 2 == 1 && k < 16) b_dma(4 + k / 2); };
    bf8 xf[2][4];
    if (type == 0) {
      f4 acc[4][6];
      bf8 wf[2][6];
      mainloop<true, 6, 4>(acc, xf, wf, L, ask, look, f8a, f8b, st1, st2);
      STAMP(0, 2); STAMPC(0, 7);
      u4 hv;
      float et[8];
      auto epi = [&](int b, int ip, int st) {
        glu_stage(st, acc[2 * ip][b], acc[2 * ip + 1][b], acc[2 * ip][b + 3], acc[2 * ip + 1][b + 3], et, hv);
        if (st == 7) BS128(hv, rs, af + (6 * c + 3 * j + b) * 2048 + ip * 1024);
      };
#define NGR 15
#pragma unroll
      for (int pr = 0; pr < 3; ++pr)
#pragma unroll
        for (int k = 0; k < 16; ++k) {
          const int hf = k >> 3, b = pr + 3 * ((k >> 2) & 1), i = k & 3, r = (pr - 1) * 8 + (k >> 1);
          if (pr < 3) acc[i][b % 6] = mfma(wf[hf][b % 6], xf[hf][i], acc[i][b % 6]);
          SB0();
          if (pr == 0 && (k & 1)) b_dma(12 + (k >> 1));
          if (pr > 0) epi(pr - 1, hf, k & 7);
          if (pr > 0 && !(k & 1) && r < 15) b_reg_if(r, false);
          MEMB(); SB0();
        }
#pragma unroll
      for (int ip = 0; ip < 2; ++ip)
        BS128(glu_packed(acc[2 * ip][2], acc[2 * ip + 1][2], acc[2 * ip][5], acc[2 * ip + 1][5]), rs, af + (6 * c + 3 * j + 2) * 2048 + ip * 1024);
      STAMP(0, 3);
#define NPRE 6
      VM0C();
      VMCNT(0);
#pragma unroll
      for (int k = 0; k < NPRE; ++k) b_reg_if(k, true);
      BARRIER();
      flag_store(rs, tid, FL_G + rt * 18 + c, tag);
#pragma unroll
      for (int k = NPRE; k < 15; ++k) b_reg_if(k, true);
      setup2();
      hv0 = ~tag;
    } else if (type == 2) {
      f4 acc[4][5];
      bf8 wf[2][5];
      mainloop<true, 5, 4>(acc, xf, wf, L, ask, look, f8a, f8b, st1, st2);
      STAMP(0, 2); STAMPC(0, 7);
      u2 gr[4];
      const int row0 = rt * 256 + wm * 64, kidx = FL_KV + (lane & 7) * 8 + 2 * c + ((lane >> 3) & 1);
      u2 pq[4][6];
      float et[16], pt[16];
      u32 ot[8];
      gate_load(rs, row0, r16, gr);
#pragma unroll
      for (int b = 0; b < 6; ++b)
#pragma unroll
        for (int k = 0; k < 8; ++k) {
          if (b < 5) acc[k & 3][b % 5] = mfma(wf[k >> 2][b % 5], xf[k >> 2][k & 3], acc[k & 3][b % 5]);
          SB0();
          if (b == 0) b_dma(12 + k);
          else {
            phi_stage(k, acc[0][b - 1], acc[1][b - 1], acc[2][b - 1], acc[3][b - 1], et, pt, ot);
            if (k == 7) {
#pragma unroll
              for (int i = 0; i < 4; ++i) {
                pq[i][b - 1] = u2{ot[2 * i], ot[2 * i + 1]};
                acc[i][b - 1] = f4{et[4 * i], et[4 * i + 1], et[4 * i + 2], et[4 * i + 3]};
              }
            }
#define QB 1
            const int r = (b - QB) * 4 + (k >> 1);
            if (!(k & 1) && r >= 0 && r < 15) b_regs(r, r + 1);
          }
          MEMB(); SB0();
        }
      b_regs((6 - QB) * 4 < 15 ? (6 - QB) * 4 : 15, 15);
#pragma unroll
      for (int i = 0; i < 4; ++i) pq[i][5] = u2{0u, 0u};
      gate_wait(rs, row0, r16, tag, gr);
      const int zq = lane - 10 * (lane >= 10);
      u32 zsrc = pick(lane < 20, (lane >= 10) * 384 + (zq >> 2) * 128 + (zq & 3) * 16, OOB), srcl[5];
#pragma unroll
      for (int o = 0; o < 5; ++o) { srcl[o] = pick((4 * o + g) / 5 == (r16 & 3), 4 * o + g, 63) * 4; asm volatile("" : "+v"(srcl[o])); }
      asm volatile("" : "+v"(zsrc));
      setup2();
      STAMP(1, 7);
      if (w == 0) wait_flags(ws, kidx, tag, flag_load(ws, kidx));
      BARRIER();
      STAMP(1, 6);
      u4 zp[8];
#pragma unroll
      for (int r = 0; r < 8; ++r) zp[r] = BL128(rs, zsrc, ZP_OFF + (r * 16 + 4 * c + 2 * j) * 384);
      f4 z = __builtin_bit_cast(f4, zp[0]);
#pragma unroll
      for (int r = 1; r < 8; ++r) z += __builtin_bit_cast(f4, zp[r]);
      const u2 zb = pk4(z);
      f4 den[4];
#pragma unroll
      for (int i = 0; i < 4; ++i) den[i] = f4{0.f, 0.f, 0.f, 0.f};
#pragma unroll
      for (int s = 0; s < 3; ++s) {
        u4 zf;
#pragma unroll
        for (int o = 0; o < 2; ++o) {
          zf[2 * o] = 2 * s + o < 5 ? __builtin_amdgcn_ds_bpermute(srcl[(2 * s + o) % 5], zb[0]) : 0;
          zf[2 * o + 1] = 2 * s + o < 5 ? __builtin_amdgcn_ds_bpermute(srcl[(2 * s + o) % 5], zb[1]) : 0;
        }
#pragma unroll
        for (int i = 0; i < 4; ++i) den[i] = mfma(zf, u4{pq[i][2 * s][0], pq[i][2 * s][1], pq[i][2 * s + 1][0], pq[i][2 * s + 1][1]}, den[i]);
      }
#pragma unroll
      for (int i = 0; i < 4; ++i)
#pragma unroll
        for (int e = 0; e < 4; ++e) den[i][e] = __builtin_bit_cast(f2, gr[i])[0] * __builtin_amdgcn_rcpf(den[i][e] + 1e-6f);
#pragma unroll
      for (int b = 0; b < 5; ++b)
#pragma unroll
        for (int ip = 0; ip < 2; ++ip) {
          f4 v[2];
#pragma unroll
          for (int o = 0; o < 2; ++o) {
            const f4 d = den[2 * ip + o];
            v[o] = acc[2 * ip + o][b] * (b == 0 ? d[0] : (b == 4 ? d[3] : (g < b ? d[(b + 3) & 3] : d[b & 3])));
          }
          BS128(pk8(v[0], v[1]), rs, af + (2 * KH + 10 * c + 5 * j + b) * 2048 + ip * 1024);
        }
      STAMP(0, 3);
      hv0 = flag_load(ws, hidx);
      VM0C(); VMCNT(0); BARRIER();
      flag_store(rs, tid, FL_S + rt * 4 + c, tag);
      asm volatile("73:");
    } else {
      f4 acc[4][5];
      bf8 wf[2][5];
      mainloop<false, 5, 0>(acc, xf, wf, L, ask, []() {}, [](int) {}, [](int) {}, st1, st2);
      look();
      STAMP(0, 2); STAMPC(0, 7);
      constexpr int PA[8] = {0, 0, 1, 1, 1, 2, 2, 2}, PB[8] = {0, 1, 0, 1, 2, 1, 2, 0};
      u4 pk[2][3], vv[2][3];
      float et[16], pt[16];
      u32 ot[8];
#pragma unroll
      for (int b = 0; b < 6; ++b)
#pragma unroll
        for (int k = 0; k < 8; ++k) {
          if (b < 5) acc[k & 3][b % 5] = mfma(xf[k >> 2][k & 3], wf[k >> 2][b % 5], acc[k & 3][b % 5]);
          SB0();
          if (b > 0 && b < 4) {
            phi_stage(k, acc[0][b - 1], acc[1][b - 1], acc[2][b - 1], acc[3][b - 1], et, pt, ot);
            if (k == 7) { pk[0][b - 1] = u4{ot[0], ot[1], ot[2], ot[3]}; pk[1][b - 1] = u4{ot[4], ot[5], ot[6], ot[7]}; }
          }
          if (b >= 4) {
#pragma unroll
            for (int vb = (b == 4 ? 2 : 4); vb < (b == 4 ? 4 : 5); ++vb) {
              const u32 v = pk2(acc[k >> 1][vb][2 * (k & 1)], acc[k >> 1][vb][2 * (k & 1) + 1]);
              vv[k >> 2][vb - 2][k & 3] = vb == 2 ? pick(r16 == 0, 0x3f803f80u, v) : v;
            }
          }
#define K0 8
#define KPLAIN
          if (b < 4 && (k % 3 == 1) && 3 * b + k / 3 < K0) b_dma(3 * b + k / 3);
          if (b < 5) SB0();
        }
#pragma unroll
      for (int n = 0; n < 8; ++n) {
        const f4 kv = mfma(vv[1][PB[n]], pk[1][PA[n]], mfma(vv[0][PB[n]], pk[0][PA[n]], f4{0.f, 0.f, 0.f, 0.f}));
        *(LAS f4*)(w * 8192 + (w >> 2) * 98304 + n * 1024 + lane * 16) = kv;
      }
      LGKM0(); BARRIER();
      const u32 zo = pick((w == 0 || w == 2 || w == 7) & (lane < 32), ((w + 1) / 3) * 128 + lane * 4, OOB);
#pragma unroll
      for (int j2 = 0; j2 < 2; ++j2) {
        f4 sum = *(LAS f4*)((j2 * 8 + w) * 1024 + lane * 16);
#pragma unroll
        for (int m = 1; m < 4; ++m) sum += *(LAS f4*)(((2 * m + j2) * 8 + w) * 1024 + (m >> 1) * 98304 + lane * 16);
        BS128(sum, rs, KVP_OFF + ((rt * 16 + 2 * c + j2) * 8 + w) * 1024 + vo);
        const float z0 = sum[0];
        __builtin_amdgcn_raw_buffer_store_b32(__builtin_bit_cast(u32, z0), rs, zo, ZP_OFF + (rt * 16 + 2 * c + j2) * 384, SC1);
      }
      STAMP(0, 3);
      VM0C(); VMCNT(0); LGKM0(); BARRIER();
      flag_store(rs, tid, FL_KV + rt * 8 + c, tag);
      asm volatile("74:");
      STAMP(1, 7);
#pragma unroll
      for (int k = K0; k < 20; ++k) b_dma(k);
      b_regs(0, 15);
      setup2();
      hv0 = flag_load(ws, hidx);
      VM0C(); VMCNT(0);
    }
  }
  STAMP(0, 4);

  u32 pcv[3] = {w * 4096 + vo, w * 4096 + vo + 2 * XBUF, w * 4096 + vo + 4 * XBUF}, vp = vo;
  asm volatile("" : "+v"(pcv[0]), "+v"(pcv[1]), "+v"(pcv[2]), "+v"(vp));
  auto pc_addr = [&](int k) { return pcv[pc_base(k) >> 16] + (pc_base(k) & 0xffff); };
  const u32 grow = GS_OFF + (rt2 * 64 + (w >> 1) * 16) * 8;
  u2 ge;
  {
#define OW 4
    constexpr int RA = 4;
    static_assert(OW == 4, "");
    f4 acc[4][5];
    u4 ar[RA][4], bj[3];
    u32 sa[10];
#pragma unroll
    for (int s = 0; s < 10; ++s) sa[s] = s < 7 ? pick(s == 6 && nh == 6, OOB, abase + (t0 + s) * 4096) : abase + (o0 + s - 7) * 4096;
    auto lda1 = [&](int s, int i) { ar[s % RA][i] = BL128(rs, vp + i * 1024, sa[s]); };
    auto rdb = [&](int f) {
      if (f < 20) bj[f % 3] = *(LAS u4*)pc_addr(f);
      else if (f >= 35 && f < 50) {
        const int js = (f - 35) / 5, n = f % 5;
        const u32 a = n < 4 ? pc_addr(4 * js + (n >> 1)) + 8 * (n & 1) : pc_addr(12 + js) - (vp >> 1);
        const u2 lo = *(LAS u2*)a, hi = *(LAS u2*)(a + (n < 4 ? 2048 : 512));
        bj[f % 3] = u4{lo[0], lo[1], hi[0], hi[1]};
      }
    };
    auto oload = [&](int q) {
      const int js = q / 5, pp = q % 5;
      dma16n(pp & 3, pc_base(pp < 4 ? 4 * js : 12 + js) + w * 4096, vp, wssrd, bbase + (o0 + js) * 5120 + (pp & 4) * 1024);
    };
    rdb(0); rdb(1);
    if (w == 0) wait_flags(ws, hidx, tag, hv0);
    BARRIER();
    STAMP(1, 0);
#pragma unroll
    for (int s = 0; s < RA - 1; ++s) {
#pragma unroll
      for (int i = 0; i < 4; ++i) lda1(s, i);
      SB0();
    }
    ge = BL64(rs, (vp & 0xf0) >> 1, grow);
    MEMB(); SB0();
#pragma unroll
    for (int q = 0; q < 4; ++q) unpair1(ar[0], q);
    asm volatile("s_nop 1");
    u32 fl = 0;
    auto step = [&](auto S, auto LAST) __attribute__((always_inline)) {
      constexpr int s = S.v;
      constexpr bool last = LAST.v;
      if (s == OW) { wait_flags(ws, oidx, tag, fl); STAMP(1, 2); }
      SB0();
#pragma unroll
      for (int k = 0; k < 20; ++k) {
        const int n = k >> 2, i = k & 3;
        const u4 bop = (s >= 4 && s < 7) ? breg[(s + 2) % 3][n] : bj[(5 * s + n) % 3];
        mma_in(acc[i][n], bop, ar[s % RA][opix(i)], s == 0);
        SB0();
        if (s == 6 && k == 0) { VM0C(); VMCNT(0); }
        if (s == 7 && k == 11)
          asm volatile("s_waitcnt vmcnt(4)\n\ts_cmp_eq_u32 %0, 3\n\ts_cbranch_scc1 1f\n\ts_waitcnt vmcnt(0)\n1:" ::"s"(no) : "memory", "scc");
        if (s == 8 && !last && k == 11) VMCNT(0);
        if (i == 0 && !(last && n >= 3)) rdb(5 * s + n + 2);
        const int sn = s + RA - 1;
        if (s != 5 && k % 5 == 1 && sn < 9) lda1(sn, k / 5);
        if (k == 3 && s == OW - 1) fl = flag_load(ws, oidx);
        if (s == 4 && k % 5 == 3 && k < 15) oload(k / 5);
        if (s == 5 && k < 12 && !(k & 1)) { if (k == 2 || k == 6) oload(3 + k / 4); else lda1(8, k == 10 ? 3 : k / 4); }
        if (s == 6 && k % 4 == 1) oload(5 + k / 4);
        if (s == 6 && k % 4 == 3 && k < 16)
          asm volatile("s_cmp_eq_u32 %4, 2\n\ts_cbranch_scc1 1f\n\tbuffer_load_dwordx4 %0, %1, %2, %3 offen offset:%5 sc1\n1:"
                       : "=&v"(ar[1][(k / 4) & 3]) : "v"(vp), "s"(wssrd), "s"(sa[9]), "s"(no), "i"(((k / 4) & 3) * 1024) : "memory", "scc");
        if (s == 7 && k % 3 == 1 && k < 15) {
          const int q = k / 3;
          asm volatile("s_cmp_eq_u32 %4, 2\n\ts_cbranch_scc1 1f\n\ts_mov_b32 m0, %0\n\ts_nop 0\n\tbuffer_load_dwordx4 %1, %2, %3 offen offset:%5 sc1 lds\n1:"
                       ::"s"(pc_base(q < 4 ? 8 : 14) + w * 4096), "v"(vp), "s"(wssrd), "s"(bbase + (o0 + 2) * 5120 + (q < 4 ? 0 : 4096)), "s"(no), "i"((q & 3) * 1024)
                       : "memory", "scc");
        }
        MEMB();
        if (!last) {
          if (k >= 12 && k < 16) unpair1(ar[(s + 1) % RA], k - 12);
        } else if (n >= 1) *(LAS f4*)pc_addr(((0x31240 >> (4 * (n - 1))) & 15) * 4 + i) = acc[i][n - 1];
        SB0();
      }
      if (last || s == 7 || s == OW - 1) asm volatile("s_nop 7" ::: "memory");
      SB0();
      if (last) {
#pragma unroll
        for (int i = 0; i < 4; ++i) *(LAS f4*)pc_addr(12 + i) = acc[i][4];
      }
    };
    step(IC<0>{}, IC<0>{}); step(IC<1>{}, IC<0>{}); step(IC<2>{}, IC<0>{}); step(IC<3>{}, IC<0>{}); step(IC<4>{}, IC<0>{});
    step(IC<5>{}, IC<0>{}); step(IC<6>{}, IC<0>{}); step(IC<7>{}, IC<0>{});
    if (no == 2) step(IC<8>{}, IC<1>{});
    int no2 = no;
    asm volatile("" : "+s"(no2));
    if (no2 == 3) { step(IC<8>{}, IC<0>{}); step(IC<9>{}, IC<1>{}); }
    STAMP(1, 3);
  }
  for (u32 spin = 0; !__all(ge[1] == tag) && spin < SPIN_CAP; ++spin) {
    __builtin_amdgcn_s_sleep(2);
    MEMB();
    ge = BL64(rs, (vp & 0xf0) >> 1, grow);
  }
  const float om = __builtin_amdgcn_rcpf(1.f + __builtin_bit_cast(f2, ge)[0]);
  STAMP(1, 4);
  asm volatile("72:");
  LGKM0(); BARRIER();
  {
    const int i = w >> 1, n0 = (w & 1) * 3;
    u16* op = out + (rt2 * 64 + i * 16 + ((vp >> 4) & 15)) * D_ + ct * 80 + n0 * 16 + ((vp >> 8) << 2);
#pragma unroll
    for (int n = 0; n < 3; ++n) {
      if (n0 + n < 5) {
        const u32 a = ((0x43201u >> (4 * (n0 + n))) & 15) * XBUF + i * 1024 + vp;
        f4 v = *(LAS f4*)a;
#pragma unroll
        for (int s = 1; s < 8; ++s) v += *(LAS f4*)(a + s * 4096);
        *(u2*)(op + n * 16) = pk4(v * om);
      }
    }
  }
  STAMP(1, 5);
}

extern "C" void kernel_launch(void* const* d_in, const int* in_sizes, int n_in, void* d_out, int out_size, void* d_ws, size_t ws_size,
                              hipStream_t stream) {
  (void)in_sizes; (void)n_in; (void)out_size; (void)ws_size;
  (void)hipFuncSetAttribute((const void*)fused, hipFuncAttributeMaxDynamicSharedMemorySize, LDS_BYTES);
  fused<<<256, 512, LDS_BYTES, stream>>>((const u16*)d_in[0], (const u16*)d_in[1], (const u16*)d_in[2], (const u16*)d_in[4], (const u16*)d_in[5],
                                         (const u16*)d_in[6], (const u16*)d_in[7], (char*)d_ws, (u16*)d_out);
}
```

```cpp
#include <hip/hip_runtime.h>
#include <stdint.h>

typedef unsigned short u16;
typedef unsigned int u32;
typedef unsigned long long u64;
typedef short s4 __attribute__((ext_vector_type(4)));
typedef __bf16 bfv8 __attribute__((ext_vector_type(8)));
typedef __bf16 bfv2 __attribute__((ext_vector_type(2)));
typedef float f2 __attribute__((ext_vector_type(2)));
typedef float f4 __attribute__((ext_vector_type(4)));
typedef u32 u4 __attribute__((ext_vector_type(4)));
typedef u32 u2 __attribute__((ext_vector_type(2)));
#define LAS __attribute__((address_space(3)))
#define DEV __device__ __forceinline__
#define SB0() __builtin_amdgcn_sched_barrier(0)

constexpr int S_ = 2048, D_ = 640, F_ = 1728;
constexpr int KS2 = 74, KH = 54;
constexpr u32 AF_OFF = 0, AF_BYTES = 32u * KS2 * 4096u;
constexpr u32 BF_OFF = AF_OFF + AF_BYTES, BF_BYTES = 8u * KS2 * 5120u;
constexpr u32 KVP_OFF = BF_OFF + BF_BYTES, KVP_BYTES = 8u * 16u * 8192u;
constexpr u32 ZP_OFF = KVP_OFF + KVP_BYTES, ZP_BYTES = 8u * 16u * 384u;
constexpr u32 GS_OFF = ZP_OFF + ZP_BYTES, GS_BYTES = 2048u * 8u;
constexpr u32 FLAG_OFF = GS_OFF + GS_BYTES, FLAG_BYTES = 529u * 128u;
constexpr int FL_R = 0, FL_G = 256, FL_KV = 400, FL_Q = 464, FL_M = 496, FL_TA = 512, FL_EPOCH = 528;
constexpr u32 WS_BYTES = FLAG_OFF + FLAG_BYTES;

constexpr u32 XBUF = 32768, WRING = 3 * XBUF, WSLOT = 12288, WPAD = 8192, LDS_BYTES = 163840;
constexpr u32 RPAD = WRING + 2 * WSLOT, SPARE = WRING + XBUF + 2 * WSLOT;
constexpr u32 OOB = 0x40000000u;
constexpr int SC1 = 16;
typedef __amdgpu_buffer_rsrc_t rsrc_t;
#define BL128(rs, vo, so) __builtin_amdgcn_raw_buffer_load_b128(rs, vo, so, SC1)
#define BL64(rs, vo, so) __builtin_amdgcn_raw_buffer_load_b64(rs, vo, so, SC1)
#define BS128(v, rs, off) __builtin_amdgcn_raw_buffer_store_b128(__builtin_bit_cast(u4, v), rs, off, 0, SC1)

DEV u4 make_srd(const void* p, u32 bytes) {
  u64 a = (u64)p;
  u4 r = {(u32)a, (u32)(a >> 32) & 0xffffu, bytes, 0x00020000u};
  return r;
}
DEV void dma16(u32 lds, u32 voff, u4 srd, u32 soff) {
  asm volatile("s_mov_b32 m0, %0\n\ts_nop 0\n\tbuffer_load_dwordx4 %1, %2, %3 offen lds" ::"s"(lds), "v"(voff), "s"(srd), "s"(soff) : "memory");
}
template <int OFF> DEV void dma16o(u32 lds, u32 voff, u4 srd, u32 soff) {
  asm volatile("s_mov_b32 m0, %0\n\ts_nop 0\n\tbuffer_load_dwordx4 %1, %2, %3 offen offset:%4 sc1 lds" ::"s"(lds), "v"(voff), "s"(srd), "s"(soff), "i"(OFF) : "memory");
}
DEV void dma16n(int d, u32 lds, u32 voff, u4 srd, u32 soff) {
  if (d == 0) dma16o<0>(lds, voff, srd, soff);
  else if (d == 1) dma16o<1024>(lds, voff, srd, soff);
  else if (d == 2) dma16o<2048>(lds, voff, srd, soff);
  else dma16o<3072>(lds, voff, srd, soff);
}
DEV void dma4c(u32 lds, u32 voff, u4 srd, u32 soff) {
  asm volatile("s_mov_b32 m0, %0\n\ts_nop 0\n\tbuffer_load_dword %1, %2, %3 offen sc1 lds" ::"s"(lds), "v"(voff), "s"(srd), "s"(soff) : "memory");
}
DEV s4 trread(u32 a) { return __builtin_amdgcn_ds_read_tr16_b64_v4i16((LAS s4*)a); }
DEV bfv8 cat8(s4 lo, s4 hi) { return __builtin_bit_cast(bfv8, __builtin_shufflevector(lo, hi, 0, 1, 2, 3, 4, 5, 6, 7)); }
DEV f4 mfma(bfv8 a, bfv8 b, f4 c) { return __builtin_amdgcn_mfma_f32_16x16x32_bf16(a, b, c, 0, 0, 0); }
DEV f4 mfma(u4 a, u4 b, f4 c) { return mfma(__builtin_bit_cast(bfv8, a), __builtin_bit_cast(bfv8, b), c); }
DEV u32 pk2(float a, float b) { f2 v = {a, b}; return __builtin_bit_cast(u32, __builtin_convertvector(v, bfv2)); }
DEV u2 pk4(f4 v) { u2 r = {pk2(v[0], v[1]), pk2(v[2], v[3])}; return r; }
DEV u4 pk8(f4 a, f4 b) { const u2 x = pk4(a), y = pk4(b); return u4{x[0], x[1], y[0], y[1]}; }
DEV u32 pick(bool c, u32 a, u32 b) { return c ? a : b; }
constexpr float LOG2E = 1.44269504089f;
DEV void phi_stage(int st, f4 t0, f4 t1, f4 t2, f4 t3, float (&e)[16], float (&p)[16], u32 (&o)[8]) {
  const f4 t[4] = {t0, t1, t2, t3};
#pragma unroll
  for (int q = 0; q < 16; ++q) {
    const float tq = t[q >> 2][q & 3];
    if (st == (q >> 3)) e[q] = __builtin_fmaf(tq, LOG2E, -1e-6f * LOG2E);
    if (st == 1 + (q >> 2)) e[q] = __builtin_amdgcn_fmed3f(__builtin_amdgcn_exp2f(e[q]), 0.f, 1.f);
    if (st == (q < 4 ? 0 : 1 + (q >> 2))) p[q] = __builtin_amdgcn_fmed3f(tq, 1e-6f, __builtin_inff());
    if (st == 5 + (q >> 3)) { e[q] += p[q]; asm volatile("" : "+v"(e[q])); }
    if (st == 7 && (q & 1)) o[q >> 1] = pk2(e[q - 1], e[q]);
  }
}
DEV u4 glu_packed(f4 g0, f4 g1, f4 u0, f4 u1) {
  f2 e[4] = {f2{g0[0], g0[1]}, f2{g0[2], g0[3]}, f2{g1[0], g1[1]}, f2{g1[2], g1[3]}};
  const f2 u[4] = {f2{u0[0], u0[1]}, f2{u0[2], u0[3]}, f2{u1[0], u1[1]}, f2{u1[2], u1[3]}};
  u4 o;
#pragma unroll
  for (int q = 0; q < 4; ++q) e[q] *= -LOG2E;
#pragma unroll
  for (int q = 0; q < 4; ++q) e[q] = f2{__builtin_amdgcn_exp2f(e[q][0]), __builtin_amdgcn_exp2f(e[q][1])};
#pragma unroll
  for (int q = 0; q < 4; ++q) e[q] += 1.f;
#pragma unroll
  for (int q = 0; q < 4; ++q) e[q] = f2{__builtin_amdgcn_rcpf(e[q][0]), __builtin_amdgcn_rcpf(e[q][1])};
#pragma unroll
  for (int q = 0; q < 4; ++q) { e[q] *= u[q]; o[q] = pk2(e[q][0], e[q][1]); }
  return o;
}
DEV void glu_stage(int st, f4 g0, f4 g1, f4 u0, f4 u1, float (&e)[8], u4& o) {
  const f4 gg[2] = {g0, g1}, uu[2] = {u0, u1};
#pragma unroll
  for (int q = 0; q < 8; ++q) {
    if (st == 0) { e[q] = gg[q >> 2][q & 3] * -LOG2E; asm volatile("" : "+v"(e[q])); }
    if (st == 1 + (q >> 2)) e[q] = __builtin_amdgcn_exp2f(e[q]);
    if (st == 3) { e[q] += 1.f; asm volatile("" : "+v"(e[q])); }
    if (st == 4 + (q >> 2)) e[q] = __builtin_amdgcn_rcpf(e[q]);
    if (st == 6) { e[q] *= uu[q >> 2][q & 3]; asm volatile("" : "+v"(e[q])); }
    if (st == 7 && (q & 1)) o[q >> 1] = pk2(e[q - 1], e[q]);
  }
}
#define MEMB() asm volatile("" ::: "memory")
#define VM0C() __builtin_amdgcn_s_waitcnt(0x0F70)
#define LGKM0() __builtin_amdgcn_s_waitcnt(0xC07F)
#define VMCNT(n) asm volatile("s_waitcnt vmcnt(" #n ")" ::: "memory")
#define BARRIER() do { asm volatile("" ::: "memory"); __builtin_amdgcn_s_barrier(); asm volatile("" ::: "memory"); } while (0)

DEV u32 flag_load(char* ws, int idx) {
  asm volatile("" ::: "memory");
  return __builtin_amdgcn_raw_buffer_load_b32(__builtin_amdgcn_make_buffer_rsrc(ws, 0, WS_BYTES, 0x00020000), idx * 128, FLAG_OFF, SC1);
}
DEV void flag_store(__amdgpu_buffer_rsrc_t rs, u32 id, int idx, u32 v) {
  __builtin_amdgcn_raw_buffer_store_b32(v, rs, id < 32 ? id * 4 : OOB, FLAG_OFF + idx * 128, SC1);
  asm volatile("" ::: "memory");
}
constexpr u32 SPIN_CAP = 1u << 19;
DEV void wait_flags(char* ws, int idx, u32 tag, u32 v) {
  for (u32 spin = 0; !__all(v == tag) && spin < SPIN_CAP; ++spin) {
    __builtin_amdgcn_s_sleep(2);
    v = flag_load(ws, idx);
    asm volatile("" : "+v"(v));
  }
  asm volatile("" ::: "memory");
}
DEV u32 load_epoch(char* ws) {
  u32 e;
  asm volatile("s_load_dword %0, %1, 0x0\n\ts_waitcnt lgkmcnt(0)" : "=s"(e) : "s"(ws + FLAG_OFF + FL_EPOCH * 128) : "memory");
  return e;
}
DEV void gate_load(rsrc_t rs, int row0, int r16, u2 (&gr)[4]) {
#pragma unroll
  for (int i = 0; i < 4; ++i) gr[i] = BL64(rs, r16 * 8 + i * 128, GS_OFF + row0 * 8);
}
DEV void gate_wait(rsrc_t rs, int row0, int r16, u32 tag, u2 (&gr)[4]) {
  for (u32 spin = 0; !__all(gr[0][1] == tag && gr[1][1] == tag && gr[2][1] == tag && gr[3][1] == tag) && spin < SPIN_CAP; ++spin) {
    __builtin_amdgcn_s_sleep(2);
    asm volatile("" ::: "memory");
    gate_load(rs, row0, r16, gr);
    asm volatile("" : "+v"(gr[0]), "+v"(gr[1]), "+v"(gr[2]), "+v"(gr[3]));
  }
}

struct Loop {
  u4 xsrd, wsrd;
  u32 xso, wso, wpitch64;
  u32 xvo[4], wvo[3];
  u32 xdst, wdst;
  u32 xrd[2], wrd[5];
};
struct St { u32 xa, wa[5], rx, dw, dx, sw, sx; };
template <int NB> DEV u32 waddr(const u32 (&wa)[5], int b) { return NB == 6 ? wa[b % 3] + (b / 3) * 6144 : wa[b]; }

template <bool TR, int NB, int H, int MODE, class FN>
DEV void half_step(f4 (&acc)[4][NB], const bfv8 (&xc)[4], const bfv8 (&wc)[NB], bfv8 (&xn)[4], bfv8 (&wn)[NB], const Loop& L, St& s, FN fn) {
  constexpr int NA = NB == 6 ? 3 : 5;
  s4 lo[NB];
#pragma unroll
  for (int k = 0; k < 4 * NB; ++k) {
    const int b = k >> 2, i = k & 3;
    acc[i][b] = TR ? mfma(wc[b], xc[i], acc[i][b]) : mfma(xc[i], wc[b], acc[i][b]);
    SB0();
    if (MODE < 2) {
      if (k < 4) xn[k] = *(LAS bfv8*)(s.xa + k * 2048);
      else if (k < 4 + 2 * NB) {
        const int nb = (k - 4) >> 1;
        const u32 a = waddr<NB>(s.wa, nb) + (H == 0 ? WSLOT : 0);
        if (k & 1) wn[nb] = cat8(lo[nb], trread(a + 768)); else lo[nb] = trread(a);
      }
      if (k == 4) {
        if (H == 0) {
          asm volatile("" : "+s"(s.rx));
          s.rx = s.rx == 2 * XBUF ? 0 : s.rx + XBUF;
        }
        s.xa = L.xrd[H] + s.rx;
        asm volatile("" : "+v"(s.xa));
      }
      if (H == 0 && k >= 4 + 2 * NB && k < 4 + 2 * NB + NA) {
        s.wa[k - 4 - 2 * NB] ^= WRING ^ (WRING + 2 * WSLOT + WPAD);
        asm volatile("" : "+v"(s.wa[k - 4 - 2 * NB]));
      }
    }
    if (MODE == 0) {
      if (H == 1 && (k & 1) && k < 6) dma16n(k >> 1, s.dw, L.wvo[(k >> 1) % 3], L.wsrd, s.sw);
      if (H == 0 && k % NB == 1) dma16n(k / NB, s.dx, L.xvo[k / NB], L.xsrd, s.sx);
      if (k == 2) {
        if (H == 0) {
          asm volatile("" : "+s"(s.dw), "+s"(s.sw));
          s.dw ^= WRING ^ (WRING + 2 * WSLOT + WPAD); s.sw += L.wpitch64;
          asm volatile("" : "+s"(s.dw), "+s"(s.sw));
        } else {
          asm volatile("" : "+s"(s.dx), "+s"(s.sx));
          s.dx = s.dx >= 2 * XBUF ? s.dx - 2 * XBUF : s.dx + XBUF; s.sx += 128;
          asm volatile("" : "+s"(s.dx), "+s"(s.sx));
        }
      }
    }
    fn(k);
    SB0();
  }
}

template <bool TR, int NB, int N8A, class REST, class ASK, class LOOK, class F0, class F1>
DEV void mainloop(f4 (&acc)[4][NB], bfv8 (&xf)[2][4], bfv8 (&wf)[2][NB], const float (&init)[NB], const Loop& L, REST rest, ASK ask, LOOK look, F0 f8a, F1 f8b) {
  constexpr int NA = NB == 6 ? 3 : 5;
  bfv8 x1[4], w1[NB];
#pragma unroll
  for (int i = 0; i < 4; ++i)
#pragma unroll
    for (int b = 0; b < NB; ++b) { acc[i][b] = f4{init[b], init[b], init[b], init[b]}; asm volatile("" : "+v"(acc[i][b])); }
  St s;
  s.rx = 0; s.xa = L.xrd[1];
#pragma unroll
  for (int b = 0; b < NA; ++b) s.wa[b] = L.wrd[b];
  s.dw = L.wdst ^ (WRING ^ (WRING + 2 * WSLOT + WPAD)); s.sw = L.wso + L.wpitch64;
  s.dx = 2 * XBUF + L.xdst; s.sx = L.xso + 2 * 128;
  SB0();
  rest();
  VMCNT(9);
  BARRIER();
#pragma unroll
  for (int i = 0; i < 4; ++i) xf[0][i] = *(LAS bfv8*)(L.xrd[0] + i * 2048);
#pragma unroll
  for (int b = 0; b < NB; ++b) { const u32 a = waddr<NB>(L.wrd, b); wf[0][b] = cat8(trread(a), trread(a + 768)); }
  auto nop = [](int) {};
#pragma unroll 1
  for (int t = 0; t < 8; ++t) {
    SB0(); LGKM0(); SB0();
    half_step<TR, NB, 0, 0>(acc, xf[0], wf[0], x1, w1, L, s, nop);
    SB0(); VMCNT(4); LGKM0(); BARRIER(); SB0();
    half_step<TR, NB, 1, 0>(acc, x1, w1, xf[0], wf[0], L, s, [&](int k) { ask(t, k); });
  }
  look();
  SB0(); LGKM0(); SB0();
  half_step<TR, NB, 0, 1>(acc, xf[0], wf[0], x1, w1, L, s, f8a);
  SB0();
  asm volatile("s_waitcnt vmcnt(%0)" ::"i"(N8A) : "memory");
  LGKM0(); BARRIER(); SB0();
  s4 lo[NB];
  half_step<TR, NB, 1, 1>(acc, x1, w1, xf[0], wf[0], L, s, [&](int k) {
    const int r = k - 2;
    if (r >= 0 && r < 2 * NB) {
      const u32 a = waddr<NB>(s.wa, r >> 1) + WSLOT;
      if (r & 1) wf[1][r >> 1] = cat8(lo[r >> 1], trread(a + 768)); else lo[r >> 1] = trread(a);
    } else if (r >= 2 * NB && r < 2 * NB + 4) xf[1][r - 2 * NB] = *(LAS bfv8*)(L.xrd[1] + (r - 2 * NB) * 2048);
    f8b(k);
  });
  SB0(); LGKM0(); BARRIER(); SB0();
}

DEV constexpr u32 pc_base(int k) { return (k < 4 ? XBUF : k < 8 ? WRING : k < 12 ? 2 * XBUF : k < 16 ? WRING + XBUF : 0) + (k & 3) * 1024; }

DEV void unpair1(u4 (&p)[4], int q) {
  const int ip = q >> 1, e = q & 1;
  u32 x = p[ip][2 + e], y = p[2 + ip][e];
  asm volatile("v_swap_b32 %0, %1" : "+v"(x), "+v"(y));
  p[ip][2 + e] = x; p[2 + ip][e] = y;
}
DEV void mma_in(f4& c, u4 a, u4 b, bool first) {
  if (first) asm volatile("v_mfma_f32_16x16x32_bf16 %0, %1, %2, 0" : "=&v"(c) : "v"(a), "v"(b));
  else asm volatile("v_mfma_f32_16x16x32_bf16 %0, %1, %2, %0" : "+v"(c) : "v"(a), "v"(b));
}
DEV constexpr int opix(int i) { return 2 * (i & 1) + (i >> 1); }
template <int V> struct IC { static constexpr int v = V; };

__global__ __launch_bounds__(512, 2) void fused(const u16* __restrict__ x, const u16* __restrict__ wqkv, const u16* __restrict__ wo,
                                                const u16* __restrict__ wg, const u16* __restrict__ wu, const u16* __restrict__ wd,
                                                const u16* __restrict__ gw, char* ws, u16* __restrict__ out) {
  extern __shared__ __attribute__((aligned(16))) char smem[];
  int lane = threadIdx.x & 63, w = __builtin_amdgcn_readfirstlane(threadIdx.x >> 6), bid = (blockIdx.x + 240) & 255;
  asm volatile("" ::"s"(x), "s"(wqkv), "s"(wo), "s"(wg), "s"(wu), "s"(wd), "s"(gw), "s"(ws), "s"(out));
  {
    u64 pc0;
    u32 clen;
    asm volatile("s_getpc_b64 %0\n71:\n\ts_mov_b32 %1, 72f-71b\n\ts_max_u32 %1, %1, 73f-71b\n\ts_max_u32 %1, %1, 74f-71b" : "=s"(pc0), "=s"(clen)::"scc");
    if (bid >= 240 && bid < 248) { asm volatile("s_nop 4"); dma4c(SPARE + 4096 + w * 256, lane * 128, make_srd((const void*)pc0, clen), w * 8192); }
  }
  Loop L;
  int rt = 0, cq = 0, type = 0, c = 0;

  if (bid < 240) {
    const int xcd = bid & 7, jj = bid >> 3;
    const int idx = (xcd >> 1) * 30 + jj;
    rt = (xcd & 1) * 4 + (idx & 3); cq = idx >> 2;
    L.xsrd = make_srd(x, S_ * D_ * 2);
    L.xso = (rt * 256 + w * 32) * 1280;
#pragma unroll
    for (int i = 0; i < 4; ++i) L.xvo[i] = (lane >> 3) * 1280 + (((lane & 7) ^ ((4 * i + (lane >> 4)) & 7)) << 4) + i * (10240 - 1024);
    L.xdst = w * 4096;
    asm volatile("s_nop 4");
#pragma unroll
    for (int i = 0; i < 4; ++i) dma16n(i, L.xdst, L.xvo[i], L.xsrd, L.xso);
    asm volatile("" : "+s"(cq), "+s"(w)); asm volatile("" : "+v"(lane));
    __builtin_assume(w >= 0 && w < 8 && lane >= 0 && lane < 64 && cq >= 0 && cq < 30);
    const int c15 = cq >= 15 ? cq - 15 : cq, up = cq >= 15;
    const int r8 = c15 & 7, h8 = c15 >> 3;
    type = (0x00000211 >> (4 * r8)) & 15;
    const int ord = (0x43210010 >> (4 * r8)) & 15;
    c = up * pick(type == 0, 9, 4 >> ((type - 1) & 1)) + h8 * pick(type == 0, 5, 2 >> ((type - 1) & 1)) + ord;
    c ^= type & 2;
    {
      const int part = w >> 2, wq = w & 3;
      const u32 pitch = type == 0 ? F_ * 2 : 3 * D_ * 2;
      const u32 colbase = pick(type == 0, 96 * c, pick(type == 1, 640 + 640 * part + 80 * c, 160 * c + 80 * part));
      const int nch = type == 0 ? 12 : 10;
      L.wsrd = make_srd(type == 0 ? (part ? wu : wg) : wqkv, D_ * pitch);
      L.wpitch64 = 64 * pitch;
      L.wso = 16 * wq * pitch + colbase * 2;
#pragma unroll
      for (int i = 0; i < 3; ++i) {
        const int n = i * 64 + lane, row = n / 12, ch = (n % 12) ^ (((row >> 3) & 1) << 1);
        L.wvo[i] = pick(ch < nch, row * pitch + (ch << 4) - i * 1024, OOB);
      }
      L.wdst = WRING + (wq >> 1) * WSLOT + part * 6144 + (wq & 1) * 3072;
    }
    asm volatile("s_nop 4");
#pragma unroll
    for (int i = 0; i < 3; ++i) dma16n(i, L.wdst, L.wvo[i], L.wsrd, L.wso);
  }
  asm volatile("" : "+s"(bid), "+s"(w)); asm volatile("" : "+v"(lane));
  __builtin_assume(w >= 0 && w < 8 && lane >= 0 && lane < 64 && bid >= 0 && bid < 256);
  const int tid = w * 64 + lane, xcd = bid & 7, jj = bid >> 3;
  const int g = lane >> 4, r16 = lane & 15;
  const rsrc_t rs = __builtin_amdgcn_make_buffer_rsrc(ws, 0, WS_BYTES, 0x00020000);
  const u4 wssrd = make_srd(ws, WS_BYTES);
  const u32 vo = lane * 16;
  const int rt2 = ((~xcd & 1) * 4 + (xcd >> 1)) * 4 + (jj >> 3), ct = jj & 7;
  const u32 abase = AF_OFF + rt2 * (KS2 * 4096u), bbase = BF_OFF + ct * (KS2 * 5120u);
  const int t0 = 7 * w - (w == 7), nh = 7 - (w >= 6), o0 = KH + pick(w < 4, 3 * w, 2 * w + 4), no = 2 + (w < 4);
  u4 breg[3][5];
  auto b_dma = [&](int k) { dma16n(k & 3, pc_base(k & ~3) + w * 4096, vo, wssrd, bbase + t0 * 5120 + (k & ~3) * 1024); };
  auto b_regs = [&](int k0, int k1) {
    const u32 sb = bbase + (t0 + 4) * 5120;
#pragma unroll
    for (int k = k0; k < k1; ++k) breg[k / 5][k % 5] = BL128(rs, vo + (k & 3) * 1024, pick(k >= 10 && nh == 6, OOB, sb + (k >> 2) * 4096));
  };
  const u4 wdsrd = make_srd(wd, F_ * D_ * 2);
  auto r_in = [&](int u, u32 lds) {
    const int row = 4 * w + g;
    dma16(lds + w * 1024, row * 1280 + ((r16 ^ ((row & 7) << 1)) << 4), wdsrd, (u / 5) * 40960 + (u % 5) * 256);
  };
  auto r_out = [&](int u, u32 lds) {
    const int row = 4 * g + (r16 >> 2), n40 = (u % 5) * 8 + w;
    const u32 a = lds + row * 256 + (((2 * w + ((lane >> 1) & 1)) ^ ((row & 7) << 1)) << 4) + 8 * (lane & 1);
    BS128(cat8(trread(a), trread(a + 4096)), rs, vo + BF_OFF + (((n40 / 5) * KS2 + u / 5) * 5 + n40 % 5) * 1024);
  };
  const int hidx = pick(w == 0, FL_G + (rt2 >> 2) * 18 + (lane < 18 ? lane : 17), OOB / 128);
  const int ok0 = (o0 - KH) * 32, ok1 = ok0 + no * 32 - 1;
  int oidx;
  auto setup2 = [&]() {
    u32 l = vo >> 4;
    asm volatile("" : "+v"(l));
    const int mc = ok0 / 40 + (l & 3), mc1 = ok1 / 40;
    oidx = pick(l < 4, FL_M + (mc < mc1 ? mc : mc1), FL_Q + (rt2 >> 2) * 4 + pick(l & 1, ok0, ok1) / 160);
    asm volatile("" : "+v"(oidx));
  };
  u32 tag, hv0;
  if (jj >= 30) {
    const int tw = (xcd * 2 + jj - 22) & 15;
    u32 so[5];
#pragma unroll
    for (int i = 0; i < 5; ++i) { const int n = i * 64 + lane, row = 4 * w + n / 80, p = n % 80; so[i] = row * 1280 + ((p ^ ((row & 7) << 1)) << 4); }
    const u4 wosrd = make_srd(wo, D_ * D_ * 2);
    asm volatile("s_nop 4");
#pragma unroll
    for (int i = 0; i < 8; ++i) r_in(144 + 16 * i + tw, 2 * XBUF + i * 8192);
    if (w < 5) {
#pragma unroll
      for (int s = 0; s < 2; ++s)
#pragma unroll
        for (int i = 0; i < 5; ++i) dma16(s * 25600 + w * 5120 + i * 1024, so[i], wosrd, (tw * 2 + s) * 25600);
    }
    tag = load_epoch(ws) + 1u;
    const int q = r16 >> 2, p = lane & 3;
    VMCNT(0); BARRIER();
#pragma unroll
    for (int i = 0; i < 8; ++i)
      if (i < 7 || tw < 14) r_out(144 + 16 * i + tw, 2 * XBUF + i * 8192);
    VM0C(); VMCNT(0); LGKM0(); BARRIER();
    flag_store(rs, tid, FL_R + bid, tag);
    u4 xv[16], xt[4], gv[2];
    {
      const u4* xp = (const u4*)(x + (tw * 128 + w * 16) * D_);
      gv[0] = ((const u4*)gw)[lane]; gv[1] = ((const u4*)gw)[64 + r16];
#pragma unroll
      for (int r = 0; r < 16; ++r) xv[r] = xp[r * 80 + lane];
#pragma unroll
      for (int i = 0; i < 4; ++i) xt[i] = xp[(4 * i + g) * 80 + 64 + r16];
    }
    {
      auto dot8 = [](u4 a, u4 b) {
        float s0 = 0.f;
#pragma unroll
        for (int e = 0; e < 4; ++e) { const u32 ae = a[e], be = b[e]; s0 = __builtin_amdgcn_fdot2_f32_bf16(__builtin_bit_cast(bfv2, ae), __builtin_bit_cast(bfv2, be), s0, false); }
        return s0;
      };
      float v[16];
#pragma unroll
      for (int r = 0; r < 16; ++r) v[r] = dot8(xv[r], gv[0]);
#pragma unroll
      for (int i = 0; i < 4; ++i) {
        const float tl = dot8(xt[i], gv[1]);
#pragma unroll
        for (int e = 0; e < 4; ++e) v[4 * i + e] += g == e ? tl : 0.f;
      }
#pragma unroll
      for (int b = 0; b < 4; ++b) {
        const bool hi = (lane >> b) & 1;
#pragma unroll
        for (int r = 0; r < (8 >> b); ++r) {
          const float keep = hi ? v[2 * r + 1] : v[2 * r], send = hi ? v[2 * r] : v[2 * r + 1];
          v[r] = keep + __shfl_xor(send, 1 << b);
        }
      }
      float a0 = v[0];
      a0 += __shfl_xor(a0, 16); a0 += __shfl_xor(a0, 32);
      const float gate = __builtin_amdgcn_exp2f(LOG2E * __builtin_amdgcn_fmed3f(a0, -60.f, 60.f));
      __builtin_amdgcn_raw_buffer_store_b64(u2{__builtin_bit_cast(u32, gate), tag}, rs, pick(lane < 16, lane * 8, OOB), GS_OFF + (tw * 128 + w * 16) * 8, SC1);
    }

    if (w == 0) {
      u32 v[4];
#pragma unroll
      for (int k = 0; k < 4; ++k) v[k] = flag_load(ws, FL_R + k * 64 + lane);
#pragma unroll
      for (int k = 0; k < 4; ++k) wait_flags(ws, FL_R + k * 64 + lane, tag, v[k]);
      flag_store(rs, lane, FL_TA + tw, tag);
      flag_store(rs, lane, pick(tw == 0, FL_EPOCH, OOB / 128), tag);
    }
    bfv8 wt[2][5];
#pragma unroll
    for (int s = 0; s < 2; ++s)
#pragma unroll
      for (int nb = 0; nb < 5; ++nb) {
        const int ch = 2 * (w * 5 + nb) + (p >> 1);
        s4 v[2];
#pragma unroll
        for (int t = 0; t < 2; ++t) {
          const int e0 = 8 * g + 4 * t, row = (e0 < 20 ? e0 : 0) + q;
          v[t] = trread(s * 25600 + row * 1280 + ((ch ^ ((row & 7) << 1)) << 4) + 8 * (p & 1));
          if (e0 >= 20) v[t] = s4{0, 0, 0, 0};
        }
        wt[s][nb] = cat8(v[0], v[1]);
      }
    LGKM0(); BARRIER();
    *(LAS u2*)(tid * 8) = u2{0u, 0u};
    LGKM0(); BARRIER();
#pragma unroll
    for (int k = 0; k < 16; ++k) b_dma(k);
    if (w >= 2) { b_dma(16); b_dma(17); b_dma(18); b_dma(19); }
    b_regs(0, 15);
    const int dj = 16 * ((0x22211100 >> (4 * w)) & 15) + r16, ej = 16 * ((0x02121010 >> (4 * w)) & 15) + 4 * g - 8, hh = dj >= 20;
    u32 kdst = pick((dj < 40) & (ej >= 0) & ((ej >= 20) == hh), (dj + 16 * hh) * 64 + (ej - 20 * hh) * 2, 4096 + lane * 8), mdst[3], md16[3];
#pragma unroll
    for (int sb = 0; sb < 3; ++sb) {
      const int q4 = 10 * tw + 4 * sb + g;
      const u32 st = pick(sb < 2 || g < 2, BF_OFF + (w * KS2 + KH + (q4 >> 3)) * 5120u, OOB), hf = (q4 >> 2) & 1, ln = (q4 & 3) * 16 + r16;
      mdst[sb] = st + 4096 + hf * 512 + ln * 8; md16[sb] = st + hf * 2048 + ln * 16;
      asm volatile("" : "+v"(mdst[sb]), "+v"(md16[sb]));
    }
    asm volatile("" : "+v"(kdst));
    setup2();
    VM0C();
    wait_flags(ws, FL_KV + (lane & 7) * 8 + (tw >> 1), tag, flag_load(ws, FL_KV + (lane & 7) * 8 + (tw >> 1)));
    {
      u4 pv[8];
#pragma unroll
      for (int r = 0; r < 8; ++r) pv[r] = BL128(rs, vo, pick(w < 7, KVP_OFF + ((r * 16 + tw) * 8 + w) * 1024, OOB));
      f4 sum = __builtin_bit_cast(f4, pv[0]);
#pragma unroll
      for (int r = 1; r < 8; ++r) sum += __builtin_bit_cast(f4, pv[r]);
      *(LAS u2*)kdst = pk4(sum);
    }
    LGKM0(); BARRIER();
    bfv8 ka[4];
#pragma unroll
    for (int sb = 0; sb < 4; ++sb) ka[sb] = *(LAS bfv8*)((sb * 16 + r16) * 64 + g * 16);
#pragma unroll
    for (int sb = 0; sb < 3; ++sb) {
      f4 m[5];
#pragma unroll
      for (int nb = 0; nb < 5; ++nb) {
        m[nb] = mfma(ka[sb == 2 ? 3 : sb], wt[sb >> 1][nb], f4{0.f, 0.f, 0.f, 0.f});
        if (sb == 1) m[nb] = mfma(ka[2], wt[1][nb], m[nb]);
      }
      BS128(pk8(m[0], m[1]), rs, md16[sb]);
      BS128(pk8(m[2], m[3]), rs, md16[sb] + 1024);
      __builtin_amdgcn_raw_buffer_store_b64(pk4(m[4]), rs, mdst[sb], 0, SC1);
    }

    hv0 = flag_load(ws, hidx);
    VM0C(); VMCNT(0); BARRIER();
    flag_store(rs, tid, FL_M + tw, tag);
    if (w < 2) { b_dma(16); b_dma(17); b_dma(18); b_dma(19); }
  } else {
    const int wm = w >> 1, j = w & 1;
    const int nk = pick(type == 0, rt * 18 + c, 1 << 16);
    u32 tch, tso;
    u4 tsrd;
    bool ton;
    {
      const int st = w + 2, up = c >= 9, i = (c - 9 * up) * 2 + (rt & 1), i3 = i / 3, wh = i - 3 * i3;
      const bool isx = rt >= 6 || (rt < 4 && (c & rt & 1)), isq = (rt >> 1) == 2;
      const int xu = pick(rt >= 6, 16 + c, (c & ~1) + (rt >> 1));
      ton = type == 0 && rt != 7 && (isx ? xu < pick(rt >= 6, 32, 16) : (!isq || i < 15));
      const u64 p0 = (u64)wqkv, p1 = (u64)wg, p2 = (u64)wu, p3 = (u64)x, p12 = rt < 2 ? +p1 : +p2, p012 = isq ? +p0 : +p12, pp = isx ? +p3 : +p012;
      const u32 pitch = pick(isx, 1280, pick(isq, 3840, 3456));
      tsrd = make_srd((const void*)pp, pick(ton, pick(isx, S_, D_) * pitch, 0));
      tch = lane * pitch;
      tso = pick(isx, xu * 81920 + st * 128, st * 64 * pitch + pick(isq, pick(wh == 2, 5 * (up ^ 1), 10 + 10 * wh + 5 * up) + i3, (3 * c + 1) / 2 + (rt & 1)) * 128);
      asm volatile("" : "+v"(tch));
    }
    int wt_ = w;
    asm volatile("" : "+s"(wt_));
    const u32 twhen = pick(ton, wt_ - 2, 99);
    dma4c(SPARE + 2048 + w * 256, tch, tsrd, pick(w < 2, tso, OOB));
    SB0();
    {
#pragma unroll
      for (int s = 0; s < 2; ++s) L.xrd[s] = (64 * wm + r16) * 128 + (((4 * s + g) ^ (r16 >> 1)) << 4);
      const int q = r16 >> 2, p = lane & 3;
#pragma unroll
      for (int b = 0; b < 5; ++b) {
        const int u = 2 * b + (p >> 1);
        const int ch = pick(type == 0, 6 * j + u, pick(type == 1, 5 * j + u - 5 * (u >= 5), u));
        const int part = pick(type == 0, 0, pick(type == 1, u >= 5, j));
        L.wrd[b] = WRING + part * 6144 + (8 * g + q) * 192 + ((ch ^ ((g & 1) << 1)) << 4) + 8 * (p & 1);
      }
    }
    auto rest = [&]() {
#pragma unroll
      for (int i = 0; i < 4; ++i) dma16n(i, XBUF + L.xdst, L.xvo[i], L.xsrd, L.xso + 128);
#pragma unroll
      for (int i = 0; i < 3; ++i) dma16n(i, L.wdst + XBUF, L.wvo[i], L.wsrd, L.wso + L.wpitch64);
      r_in(nk, RPAD);
      tag = load_epoch(ws) + 1u;
    };
    const u32 af = AF_OFF + (u32)(rt * 4 + wm) * (KS2 * 4096u) + vo;
    const u32 tadr = FLAG_OFF + (FL_TA + (jj & 1) * 8 + xcd) * 128;
    u32 rlds;
    {
      const int row = 4 * g + (r16 >> 2);
      rlds = RPAD + row * 256 + (((2 * w + ((lane >> 1) & 1)) ^ ((row & 7) << 1)) << 4) + 8 * (lane & 1);
      asm volatile("" : "+v"(rlds));
    }
    const int n40 = (nk % 5) * 8 + w;
    const u32 rdst = BF_OFF + (((n40 / 5) * KS2 + nk / 5) * 5 + n40 % 5) * 1024, hooks = pick(type != 0, 0x82, 0x83);
    u4 fsrd = wssrd;
    fsrd[2] = pick(w == 0, FLAG_OFF + (FL_R + bid + 1) * 128, 0u);
    auto ask = [&](int t, int k) {
      if (k == 0) {
        asm volatile("s_cmp_lg_u32 %0, %1\n\ts_cbranch_scc1 1f\n\ts_mov_b32 m0, %2\n\ts_nop 0\n\tbuffer_load_dword %3, %4, %5 offen sc1 lds\n1:"
                     ::"s"(t), "s"(twhen), "s"(SPARE + 2048 + w * 256), "v"(tch), "s"(tsrd), "s"(tso) : "memory", "scc");
        asm volatile(
            "s_bitcmp1_b32 %10, %0\n\t"
            "s_cbranch_scc0 3f\n\t"
            "s_cmp_eq_u32 %0, 1\n\t"
            "s_cbranch_scc1 1f\n\t"
            "s_cmp_eq_u32 %0, 7\n\t"
            "s_cbranch_scc1 2f\n\t"
            "ds_read_b64_tr_b16 v[252:253], %1\n\t"
            "ds_read_b64_tr_b16 v[254:255], %1 offset:4096\n\t"
            "s_waitcnt lgkmcnt(0)\n\t"
            "buffer_store_dwordx4 v[252:255], %2, %3, %4 offen sc1\n\t"
            "s_nop 1\n\t"
            "s_branch 3f\n"
            "1:\n\t"
            "v_mov_b32 v252, %7\n\t"
            "v_lshrrev_b32 v253, 2, %2\n\t"
            "buffer_store_dword v252, v253, %5, %6 offen sc1\n\t"
            "s_branch 3f\n"
            "2:\n\t"
            "s_mov_b32 m0, %8\n\t"
            "s_nop 0\n\t"
            "buffer_load_dword off, %3, %9 sc1 lds\n"
            "3:"
            :
            : "s"(t), "v"(rlds), "v"(vo), "s"(wssrd), "s"(rdst), "s"(fsrd), "s"(FLAG_OFF + (FL_R + bid) * 128), "s"(tag), "s"(SPARE + w * 256), "s"(tadr), "s"(hooks)
            : "memory", "scc", "v252", "v253", "v254", "v255");
      }
    };
    auto settle = [&](u32 v) {
      for (u32 spin = 0; __builtin_amdgcn_readfirstlane(v) != tag && spin < SPIN_CAP; ++spin) {
        __builtin_amdgcn_s_sleep(2);
        asm volatile("buffer_load_dword %0, off, %1, %2 sc1\n\ts_waitcnt vmcnt(0)" : "=v"(v) : "s"(wssrd), "s"(tadr) : "memory");
      }
    };
    auto look = [&]() { VMCNT(3); settle(*(LAS u32*)(SPARE + w * 256)); };
    auto f8a = [&](int k) { if (k % 5 == 1 && k < 20) b_dma(k / 5); };
    auto f8b = [&](int k) { if (k % 2 == 1 && k < 16) b_dma(4 + k / 2); };
    bfv8 xf[2][4];
    if (type == 0) {
      f4 acc[4][6];
      bfv8 wf[2][6];
      mainloop<true, 6, 4>(acc, xf, wf, {0.f, 0.f, 0.f, 0.f, 0.f, 0.f}, L, rest, ask, look, f8a, f8b);
      u4 hv;
      float et[8];
      auto epi = [&](int b, int ip, int st) {
        glu_stage(st, acc[2 * ip][b], acc[2 * ip + 1][b], acc[2 * ip][b + 3], acc[2 * ip + 1][b + 3], et, hv);
        if (st == 7) BS128(hv, rs, af + (6 * c + 3 * j + b) * 2048 + ip * 1024);
      };
#pragma unroll
      for (int pr = 0; pr < 3; ++pr)
#pragma unroll
        for (int k = 0; k < 16; ++k) {
          const int hf = k >> 3, b = pr + 3 * ((k >> 2) & 1), i = k & 3;
          acc[i][b] = mfma(wf[hf][b], xf[hf][i], acc[i][b]);
          SB0();
          if (pr == 0 && (k & 1)) b_dma(12 + (k >> 1));
          if (pr > 0) epi(pr - 1, hf, k & 7);
          MEMB(); SB0();
        }
#pragma unroll
      for (int ip = 0; ip < 2; ++ip)
        BS128(glu_packed(acc[2 * ip][2], acc[2 * ip + 1][2], acc[2 * ip][5], acc[2 * ip + 1][5]), rs, af + (6 * c + 3 * j + 2) * 2048 + ip * 1024);
      setup2();
      hv0 = flag_load(ws, hidx);
      VM0C(); VMCNT(0);
      b_regs(0, 3);
      BARRIER();
      flag_store(rs, tid, FL_G + rt * 18 + c, tag);
      b_regs(3, 15);
    } else if (type == 2) {
      f4 acc[4][5];
      bfv8 wf[2][5];
      mainloop<true, 5, 4>(acc, xf, wf, {1e-6f, 1e-6f, 1e-6f, 1e-6f, 1e-6f}, L, rest, ask, look, f8a, f8b);
      u2 gr[4];
      const int row0 = rt * 256 + wm * 64, kidx = FL_KV + (lane & 7) * 8 + 2 * c + j;
      u2 pq[4][6];
      float et[16], pt[16];
      u32 ot[8];
      gate_load(rs, row0, r16, gr);
#pragma unroll
      for (int b = 0; b < 6; ++b)
#pragma unroll
        for (int k = 0; k < 8; ++k) {
          if (b < 5) acc[k & 3][b % 5] = mfma(wf[k >> 2][b % 5], xf[k >> 2][k & 3], acc[k & 3][b % 5]);
          SB0();
          if (b == 0) b_dma(12 + k);
          else {
            phi_stage(k, acc[0][b - 1], acc[1][b - 1], acc[2][b - 1], acc[3][b - 1], et, pt, ot);
            if (k == 7) {
#pragma unroll
              for (int i = 0; i < 4; ++i) {
                pq[i][b - 1] = u2{ot[2 * i], ot[2 * i + 1]};
                acc[i][b - 1] = f4{et[4 * i], et[4 * i + 1], et[4 * i + 2], et[4 * i + 3]};
              }
            }
            const int r = (b - 1) * 4 + (k >> 1);
            if (!(k & 1) && r < 15) b_regs(r, r + 1);
          }
          MEMB(); SB0();
        }
#pragma unroll
      for (int i = 0; i < 4; ++i) pq[i][5] = u2{0u, 0u};
      gate_wait(rs, row0, r16, tag, gr);
      const int zq = lane - 10 * (lane >= 10);
      u32 zsrc = pick(lane < 20, (lane >= 10) * 384 + (zq >> 2) * 128 + (zq & 3) * 16, OOB), srcl[5];
#pragma unroll
      for (int o = 0; o < 5; ++o) { srcl[o] = pick((4 * o + g) / 5 == (r16 & 3), 4 * o + g, 63) * 4; asm volatile("" : "+v"(srcl[o])); }
      asm volatile("" : "+v"(zsrc));
      setup2();
      wait_flags(ws, kidx, tag, flag_load(ws, kidx));
      u4 zp[8];
#pragma unroll
      for (int r = 0; r < 8; ++r) zp[r] = BL128(rs, zsrc, ZP_OFF + (r * 16 + 4 * c + 2 * j) * 384);
      f4 z = __builtin_bit_cast(f4, zp[0]);
#pragma unroll
      for (int r = 1; r < 8; ++r) z += __builtin_bit_cast(f4, zp[r]);
      const u2 zb = pk4(z);
      f4 den[4];
#pragma unroll
      for (int i = 0; i < 4; ++i) den[i] = f4{0.f, 0.f, 0.f, 0.f};
#pragma unroll
      for (int s = 0; s < 3; ++s) {
        u4 zf;
#pragma unroll
        for (int o = 0; o < 2; ++o) {
          zf[2 * o] = 2 * s + o < 5 ? __builtin_amdgcn_ds_bpermute(srcl[(2 * s + o) % 5], zb[0]) : 0;
          zf[2 * o + 1] = 2 * s + o < 5 ? __builtin_amdgcn_ds_bpermute(srcl[(2 * s + o) % 5], zb[1]) : 0;
        }
#pragma unroll
        for (int i = 0; i < 4; ++i) den[i] = mfma(zf, u4{pq[i][2 * s][0], pq[i][2 * s][1], pq[i][2 * s + 1][0], pq[i][2 * s + 1][1]}, den[i]);
      }
#pragma unroll
      for (int i = 0; i < 4; ++i)
#pragma unroll
        for (int e = 0; e < 4; ++e) den[i][e] = __builtin_bit_cast(f2, gr[i])[0] * __builtin_amdgcn_rcpf(den[i][e] + 1e-6f);
#pragma unroll
      for (int b = 0; b < 5; ++b)
#pragma unroll
        for (int ip = 0; ip < 2; ++ip) {
          f4 v[2];
#pragma unroll
          for (int o = 0; o < 2; ++o) {
            const f4 d = den[2 * ip + o];
            v[o] = acc[2 * ip + o][b] * (b == 0 ? d[0] : (b == 4 ? d[3] : (g < b ? d[(b + 3) & 3] : d[b & 3])));
          }
          BS128(pk8(v[0], v[1]), rs, af + (2 * KH + 10 * c + 5 * j + b) * 2048 + ip * 1024);
        }

      hv0 = flag_load(ws, hidx);
      VM0C(); VMCNT(0); BARRIER();
      flag_store(rs, tid, FL_Q + rt * 4 + c, tag);
      asm volatile("73:");
    } else {
      f4 acc[4][5];
      bfv8 wf[2][5];
      u32 ans;
      mainloop<false, 5, 0>(acc, xf, wf, {1e-6f, 1e-6f, r16 < 8 ? 1e-6f : 0.f, 0.f, 0.f}, L, rest, ask, []() {}, [](int) {},
                            [&](int k) { if (k == 16) ans = *(LAS u32*)(SPARE + w * 256); });
      settle(ans);
      constexpr int PA[8] = {0, 0, 1, 1, 1, 2, 2, 2}, PB[8] = {0, 1, 0, 1, 2, 1, 2, 0};
      u4 pk[2][3], vv[2][3];
      float et[16], pt[16];
      u32 ot[8];
#pragma unroll
      for (int b = 0; b < 6; ++b)
#pragma unroll
        for (int k = 0; k < 8; ++k) {
          if (b < 5) acc[k & 3][b % 5] = mfma(xf[k >> 2][k & 3], wf[k >> 2][b % 5], acc[k & 3][b % 5]);
          SB0();
          if (b > 0 && b < 4) {
            phi_stage(k, acc[0][b - 1], acc[1][b - 1], acc[2][b - 1], acc[3][b - 1], et, pt, ot);
            if (k == 7) { pk[0][b - 1] = u4{ot[0], ot[1], ot[2], ot[3]}; pk[1][b - 1] = u4{ot[4], ot[5], ot[6], ot[7]}; }
          }
          if (b >= 4) {
#pragma unroll
            for (int vb = (b == 4 ? 2 : 4); vb < (b == 4 ? 4 : 5); ++vb) {
              const u32 v = pk2(acc[k >> 1][vb][2 * (k & 1)], acc[k >> 1][vb][2 * (k & 1) + 1]);
              vv[k >> 2][vb - 2][k & 3] = vb == 2 ? pick(r16 == 0, 0x3f803f80u, v) : v;
            }
          }
          if (b < 4 && (k % 3 == 1) && 3 * b + k / 3 < 8) b_dma(3 * b + k / 3);
          if (b < 5) SB0();
        }
#pragma unroll
      for (int n = 0; n < 8; ++n) {
        const f4 kv = mfma(vv[1][PB[n]], pk[1][PA[n]], mfma(vv[0][PB[n]], pk[0][PA[n]], f4{0.f, 0.f, 0.f, 0.f}));
        *(LAS f4*)(w * 8192 + (w >> 2) * 98304 + n * 1024 + lane * 16) = kv;
      }
      LGKM0(); BARRIER();
      const u32 zo = pick((w == 0 || w == 2 || w == 7) & (lane < 32), ((w + 1) / 3) * 128 + lane * 4, OOB);
#pragma unroll
      for (int j2 = 0; j2 < 2; ++j2) {
        f4 sum = *(LAS f4*)((j2 * 8 + w) * 1024 + lane * 16);
#pragma unroll
        for (int m = 1; m < 4; ++m) sum += *(LAS f4*)(((2 * m + j2) * 8 + w) * 1024 + (m >> 1) * 98304 + lane * 16);
        BS128(sum, rs, KVP_OFF + ((rt * 16 + 2 * c + j2) * 8 + w) * 1024 + vo);
        const float z0 = sum[0];
        __builtin_amdgcn_raw_buffer_store_b32(__builtin_bit_cast(u32, z0), rs, zo, ZP_OFF + (rt * 16 + 2 * c + j2) * 384, SC1);
      }

      VM0C(); VMCNT(0); LGKM0(); BARRIER();
      flag_store(rs, tid, FL_KV + rt * 8 + c, tag);
      asm volatile("74:");

#pragma unroll
      for (int k = 8; k < 20; ++k) b_dma(k);
      b_regs(0, 15);
      setup2();
      hv0 = flag_load(ws, hidx);
      VM0C(); VMCNT(0);
    }
  }

  u32 pcv[3] = {w * 4096 + vo, w * 4096 + vo + 2 * XBUF, w * 4096 + vo + 4 * XBUF}, vp = vo;
  asm volatile("" : "+v"(pcv[0]), "+v"(pcv[1]), "+v"(pcv[2]), "+v"(vp));
  auto pc_addr = [&](int k) { return pcv[pc_base(k) >> 16] + (pc_base(k) & 0xffff); };
  const u32 grow = GS_OFF + (rt2 * 64 + (w >> 1) * 16) * 8;
  u2 ge;
  {
    constexpr int RA = 4;
    f4 acc[4][5];
    u4 ar[RA][4], bj[3];
    u32 sa[10];
#pragma unroll
    for (int s = 0; s < 10; ++s) sa[s] = s < 7 ? pick(s == 6 && nh == 6, OOB, abase + (t0 + s) * 4096) : abase + (o0 + s - 7) * 4096;
    auto lda1 = [&](int s, int i) { ar[s % RA][i] = BL128(rs, vp + i * 1024, sa[s]); };
    auto rdb = [&](int f) {
      if (f < 20) bj[f % 3] = *(LAS u4*)pc_addr(f);
      else if (f >= 35 && f < 50) {
        const int js = (f - 35) / 5, n = f % 5;
        const u32 a = n < 4 ? pc_addr(4 * js + (n >> 1)) + 8 * (n & 1) : pc_addr(12 + js) - (vp >> 1);
        const u2 lo = *(LAS u2*)a, hi = *(LAS u2*)(a + (n < 4 ? 2048 : 512));
        bj[f % 3] = u4{lo[0], lo[1], hi[0], hi[1]};
      }
    };
    auto oload = [&](int q) {
      const int js = q / 5, pp = q % 5;
      dma16n(pp & 3, pc_base(pp < 4 ? 4 * js : 12 + js) + w * 4096, vp, wssrd, bbase + (o0 + js) * 5120 + (pp & 4) * 1024);
    };
    rdb(0); rdb(1);
    if (w == 0) wait_flags(ws, hidx, tag, hv0);
    BARRIER();

#pragma unroll
    for (int s = 0; s < RA - 1; ++s) {
#pragma unroll
      for (int i = 0; i < 4; ++i) lda1(s, i);
      SB0();
    }
    ge = BL64(rs, (vp & 0xf0) >> 1, grow);
    MEMB(); SB0();
#pragma unroll
    for (int q = 0; q < 4; ++q) unpair1(ar[0], q);
    asm volatile("s_nop 1");
    u32 fl = 0;
    auto step = [&](auto S, auto LAST) __attribute__((always_inline)) {
      constexpr int s = S.v;
      constexpr bool last = LAST.v;
      if (s == 4) wait_flags(ws, oidx, tag, fl);
      SB0();
#pragma unroll
      for (int k = 0; k < 20; ++k) {
        const int n = k >> 2, i = k & 3;
        const u4 bop = (s >= 4 && s < 7) ? breg[(s + 2) % 3][n] : bj[(5 * s + n) % 3];
        mma_in(acc[i][n], bop, ar[s % RA][opix(i)], s == 0);
        SB0();
        if (s == 6 && k == 0) { VM0C(); VMCNT(0); }
        if (s == 7 && k == 11)
          asm volatile("s_waitcnt vmcnt(4)\n\ts_cmp_eq_u32 %0, 3\n\ts_cbranch_scc1 1f\n\ts_waitcnt vmcnt(0)\n1:" ::"s"(no) : "memory", "scc");
        if (s == 8 && !last && k == 11) VMCNT(0);
        if (i == 0 && !(last && n >= 3)) rdb(5 * s + n + 2);
        const int sn = s + RA - 1;
        if (s != 5 && k % 5 == 1 && sn < 9) lda1(sn, k / 5);
        if (k == 3 && s == 3) fl = flag_load(ws, oidx);
        if (s == 4 && k % 5 == 3 && k < 15) oload(k / 5);
        if (s == 5 && k < 12 && !(k & 1)) { if (k == 2 || k == 6) oload(3 + k / 4); else lda1(8, k == 10 ? 3 : k / 4); }
        if (s == 6 && k % 4 == 1) oload(5 + k / 4);
        if (s == 6 && k % 4 == 3 && k < 16)
          asm volatile("s_cmp_eq_u32 %4, 2\n\ts_cbranch_scc1 1f\n\tbuffer_load_dwordx4 %0, %1, %2, %3 offen offset:%5 sc1\n1:"
                       : "=&v"(ar[1][(k / 4) & 3]) : "v"(vp), "s"(wssrd), "s"(sa[9]), "s"(no), "i"(((k / 4) & 3) * 1024) : "memory", "scc");
        if (s == 7 && k % 3 == 1 && k < 15) {
          const int q = k / 3;
          asm volatile("s_cmp_eq_u32 %4, 2\n\ts_cbranch_scc1 1f\n\ts_mov_b32 m0, %0\n\ts_nop 0\n\tbuffer_load_dwordx4 %1, %2, %3 offen offset:%5 sc1 lds\n1:"
                       ::"s"(pc_base(q < 4 ? 8 : 14) + w * 4096), "v"(vp), "s"(wssrd), "s"(bbase + (o0 + 2) * 5120 + (q < 4 ? 0 : 4096)), "s"(no), "i"((q & 3) * 1024)
                       : "memory", "scc");
        }
        MEMB();
        if (!last) {
          if (k >= 12 && k < 16) unpair1(ar[(s + 1) % RA], k - 12);
        } else if (n >= 1) *(LAS f4*)pc_addr(((0x31240 >> (4 * (n - 1))) & 15) * 4 + i) = acc[i][n - 1];
        SB0();
      }
      if (last || s == 7 || s == 3) asm volatile("s_nop 7" ::: "memory");
      SB0();
      if (last) {
#pragma unroll
        for (int i = 0; i < 4; ++i) *(LAS f4*)pc_addr(12 + i) = acc[i][4];
      }
    };
    step(IC<0>{}, IC<0>{}); step(IC<1>{}, IC<0>{}); step(IC<2>{}, IC<0>{}); step(IC<3>{}, IC<0>{}); step(IC<4>{}, IC<0>{});
    step(IC<5>{}, IC<0>{}); step(IC<6>{}, IC<0>{}); step(IC<7>{}, IC<0>{});
    if (no == 2) step(IC<8>{}, IC<1>{});
    int no2 = no;
    asm volatile("" : "+s"(no2));
    if (no2 == 3) { step(IC<8>{}, IC<0>{}); step(IC<9>{}, IC<1>{}); }
  }
  for (u32 spin = 0; !__all(ge[1] == tag) && spin < SPIN_CAP; ++spin) {
    __builtin_amdgcn_s_sleep(2);
    MEMB();
    ge = BL64(rs, (vp & 0xf0) >> 1, grow);
  }
  const float om = __builtin_amdgcn_rcpf(1.f + __builtin_bit_cast(f2, ge)[0]);

  asm volatile("72:");
  LGKM0(); BARRIER();
  {
    const int i = w >> 1, n0 = (w & 1) * 3;
    u16* op = out + (rt2 * 64 + i * 16 + ((vp >> 4) & 15)) * D_ + ct * 80 + n0 * 16 + ((vp >> 8) << 2);
#pragma unroll
    for (int n = 0; n < 3; ++n) {
      if (n0 + n < 5) {
        const u32 a = ((0x43201u >> (4 * (n0 + n))) & 15) * XBUF + i * 1024 + vp;
        f4 v = *(LAS f4*)a;
#pragma unroll
        for (int s = 1; s < 8; ++s) v += *(LAS f4*)(a + s * 4096);
        *(u2*)(op + n * 16) = pk4(v * om);
      }
    }
  }

}

extern "C" void kernel_launch(void* const* d_in, const int* in_sizes, int n_in, void* d_out, int out_size, void* d_ws, size_t ws_size,
                              hipStream_t stream) {
  (void)in_sizes; (void)n_in; (void)out_size; (void)ws_size;
  (void)hipFuncSetAttribute((const void*)fused, hipFuncAttributeMaxDynamicSharedMemorySize, LDS_BYTES);
  fused<<<256, 512, LDS_BYTES, stream>>>((const u16*)d_in[0], (const u16*)d_in[1], (const u16*)d_in[2], (const u16*)d_in[4], (const u16*)d_in[5],
                                         (const u16*)d_in[6], (const u16*)d_in[7], (char*)d_ws, (u16*)d_out);
}
```
